```python
import torch

def _softmax(x, axis=-1):
    return torch.softmax(x, dim=axis)
def _gelu(x):
    return torch.nn.functional.gelu(x, approximate="tanh")
def _silu(x):
    return torch.nn.functional.silu(x)
def _sigmoid(x):
    return torch.sigmoid(x)
def _layernorm(x, axis=-1, eps=1e-5):
    m = x.mean(dim=axis, keepdim=True)
    v = x.var(dim=axis, keepdim=True, unbiased=False)
    return (x - m) / torch.sqrt(v + eps)
def _scatter_add(tgt, idx, src, axis=0):
    return tgt.clone().index_add_(axis, idx, src)
def _segment_sum(src, seg, n):
    out = torch.zeros((n,) + src.shape[1:], dtype=src.dtype)
    return out.index_add_(0, seg, src)
def _pad(x, pw):
    flat = tuple(v for pair in reversed(pw) for v in pair)
    return torch.nn.functional.pad(x, flat)

kernel_name = 'parallel_sum__sliding_local_dilated__mixer_channel_mlp__prompt'

def setup_inputs():

    _w = (lambda a: (lambda t: t.to(torch.bfloat16) if t.is_floating_point() else t)(torch.as_tensor(a)))
    def _A_setup():
        B, S, D = 1, 2048, 640
        return {
            "x": _w((lambda *s: torch.randn(*s, dtype=torch.bfloat16) * 0.05)(B, S, D)),
            "wqkv": _w((lambda *s: torch.randn(*s, dtype=torch.bfloat16) * 0.05)(D, 3 * D)),
            "wo": _w((lambda *s: torch.randn(*s, dtype=torch.bfloat16) * 0.05)(D, D)),
        }
    _A_in = _A_setup()

    def _B_setup():
        B, S, D, F = 1, 2048, 640, 1536
        return {
            "x": _w((lambda *s: torch.randn(*s, dtype=torch.bfloat16) * 0.05)(B, S, D)),
            "w1": _w((lambda *s: torch.randn(*s, dtype=torch.bfloat16) * 3.2)(D, F) * (D ** -0.5)),
            "b1": _w((lambda *s: torch.randn(*s, dtype=torch.bfloat16) * 1.6)(F)),
            "w2": _w((lambda *s: torch.randn(*s, dtype=torch.bfloat16) * 0.2)(F, D) * (F ** -0.5)),
            "b2": _w((lambda *s: torch.randn(*s, dtype=torch.bfloat16) * 0.4)(D)),
        }
    _B_in = _B_setup()

    return {
        "A_x": _A_in["x"],
        "A_wqkv": _A_in["wqkv"],
        "A_wo": _A_in["wo"],
        "B_x": _B_in["x"],
        "B_w1": _B_in["w1"],
        "B_b1": _B_in["b1"],
        "B_w2": _B_in["w2"],
        "B_b2": _B_in["b2"],
    }

def reference(A_x, A_wqkv, A_wo, B_x, B_w1, B_b1, B_w2, B_b2):
    def _A_ref(x, wqkv, wo):
        B, S, D = x.shape
        H = 32; Dh = D // H
        W, dil = 8, 2
        qkv = (lambda x, *d: x.permute(*d))((x @ wqkv).reshape(B, S, 3, H, Dh), 2, 0, 3, 1, 4)
        q, k, v = qkv[0], qkv[1], qkv[2]
        scale = Dh ** -0.5
        pos = torch.arange(S)
        off = torch.arange(W) * dil
        idx = pos[:, None] - off[None, :]
        mask = idx >= 0
        idx_c = torch.where(mask, idx, 0).reshape(-1)
        kg = k[:, :, idx_c, :].reshape(B, H, S, W, Dh)
        vg = v[:, :, idx_c, :].reshape(B, H, S, W, Dh)
        logits = torch.einsum("bhsd,bhswd->bhsw", q, kg) * scale
        logits = torch.where(mask[None, None, :, :], logits, -1e9)
        attn = _softmax(logits, axis=-1)
        o = torch.einsum("bhsw,bhswd->bhsd", attn, vg)
        return (lambda x, *d: x.permute(*d))(o, 0, 2, 1, 3).reshape(B, S, D) @ wo

    def _B_ref(x, w1, b1, w2, b2):
        y = _layernorm(x, axis=-1)
        h = _gelu(torch.einsum("bsd,df->bsf", y, w1) + b1)
        return torch.einsum("bsf,fd->bsd", h, w2) + b2

    x = A_x
    xn = _layernorm(x, axis=-1)
    return x + _A_ref(xn, A_wqkv, A_wo) + _B_ref(xn, B_w1, B_b1, B_w2, B_b2)
```

```cpp
#include <hip/hip_runtime.h>
#include <hip/hip_bf16.h>
#include <stdint.h>

typedef unsigned short u16;
__device__ __forceinline__ float bf2f(u16 v) { return __uint_as_float(((unsigned)v) << 16); }
__device__ __forceinline__ u16 f2bf(float f) { unsigned u = __float_as_uint(f); u += 0x7fffu + ((u >> 16) & 1u); return (u16)(u >> 16); }

constexpr int S = 2048, D = 640, F = 1536, H = 32, DH = 20;

__global__ void ln_kernel(const u16* x, u16* xn, u16* y) {
  int row = blockIdx.x; int t = threadIdx.x;
  float v[10]; float s = 0;
  for (int i = 0; i < 10; ++i) { v[i] = bf2f(x[row * D + t + 64 * i]); s += v[i]; }
  for (int m = 32; m >= 1; m >>= 1) s += __shfl_xor(s, m);
  float mean = s / D; float q = 0;
  for (int i = 0; i < 10; ++i) { float d = v[i] - mean; q += d * d; }
  for (int m = 32; m >= 1; m >>= 1) q += __shfl_xor(q, m);
  float r = rsqrtf(q / D + 1e-5f);
  float s2 = 0;
  for (int i = 0; i < 10; ++i) { u16 b = f2bf((v[i] - mean) * r); xn[row * D + t + 64 * i] = b; v[i] = bf2f(b); s2 += v[i]; }
  for (int m = 32; m >= 1; m >>= 1) s2 += __shfl_xor(s2, m);
  float mean2 = s2 / D; float q2 = 0;
  for (int i = 0; i < 10; ++i) { float d = v[i] - mean2; q2 += d * d; }
  for (int m = 32; m >= 1; m >>= 1) q2 += __shfl_xor(q2, m);
  float r2 = rsqrtf(q2 / D + 1e-5f);
  for (int i = 0; i < 10; ++i) y[row * D + t + 64 * i] = f2bf((v[i] - mean2) * r2);
}

template <int MODE>
__global__ void gemm_kernel(const u16* A, const u16* B, int lda, int N, int K, const u16* bias, u16* C, int ldc, float* Cf) {
  __shared__ float As[16][65], Bs[16][65];
  int tx = threadIdx.x & 15, ty = threadIdx.x >> 4;
  int m0 = blockIdx.y * 64, n0 = blockIdx.x * 64;
  float acc[4][4] = {};
  for (int k0 = 0; k0 < K; k0 += 16) {
    for (int i = threadIdx.x; i < 1024; i += 256) {
      int r = i >> 4, c = i & 15; As[c][r] = bf2f(A[(m0 + r) * lda + k0 + c]);
      int kr = i >> 6, nc = i & 63; Bs[kr][nc] = bf2f(B[(k0 + kr) * N + n0 + nc]);
    }
    __syncthreads();
    for (int k = 0; k < 16; ++k) {
      float a[4], b[4];
      for (int i = 0; i < 4; ++i) { a[i] = As[k][ty * 4 + i]; b[i] = Bs[k][tx * 4 + i]; }
      for (int i = 0; i < 4; ++i) for (int j = 0; j < 4; ++j) acc[i][j] += a[i] * b[j];
    }
    __syncthreads();
  }
  for (int i = 0; i < 4; ++i) for (int j = 0; j < 4; ++j) {
    int m = m0 + ty * 4 + i, n = n0 + tx * 4 + j; float v = acc[i][j];
    if (MODE == 0) C[m * ldc + n] = f2bf(v);
    if (MODE == 1) { v += bf2f(bias[n]); float u = 0.7978845608028654f * (v + 0.044715f * v * v * v); v = 0.5f * v * (1.f + tanhf(u)); C[m * ldc + n] = f2bf(v); }
    if (MODE == 2) Cf[m * ldc + n] = v;
    if (MODE == 3) Cf[m * ldc + n] += v + bf2f(bias[n]);
  }
}

__global__ void attn_kernel(const u16* qkv, u16* o) {
  int idx = blockIdx.x * blockDim.x + threadIdx.x;
  int s = idx / H, h = idx % H;
  float q[DH]; for (int d = 0; d < DH; ++d) q[d] = bf2f(qkv[s * 1920 + h * DH + d]);
  float lg[8]; float mx = -1e30f;
  for (int w = 0; w < 8; ++w) {
    int p = s - 2 * w; float a = 0;
    if (p >= 0) { for (int d = 0; d < DH; ++d) a += q[d] * bf2f(qkv[p * 1920 + 640 + h * DH + d]); a *= 0.22360679774997896f; } else a = -1e9f;
    lg[w] = a; mx = fmaxf(mx, a);
  }
  float sum = 0; for (int w = 0; w < 8; ++w) { lg[w] = __expf(lg[w] - mx); sum += lg[w]; }
  float inv = 1.f / sum;
  for (int d = 0; d < DH; ++d) {
    float a = 0;
    for (int w = 0; w < 8; ++w) { int p = s - 2 * w; if (p >= 0) a += lg[w] * bf2f(qkv[p * 1920 + 1280 + h * DH + d]); }
    o[s * D + h * DH + d] = f2bf(a * inv);
  }
}

__global__ void final_kernel(const u16* x, const float* acc, u16* out) {
  int i = blockIdx.x * blockDim.x + threadIdx.x;
  out[i] = f2bf(bf2f(x[i]) + acc[i]);
}

extern "C" void kernel_launch(void* const* d_in, const int* in_sizes, int n_in,
                              void* d_out, int out_size, void* d_ws, size_t ws_size,
                              hipStream_t stream) {
  const u16* x = (const u16*)d_in[0]; const u16* wqkv = (const u16*)d_in[1]; const u16* wo = (const u16*)d_in[2];
  const u16* w1 = (const u16*)d_in[4]; const u16* b1 = (const u16*)d_in[5]; const u16* w2 = (const u16*)d_in[6]; const u16* b2 = (const u16*)d_in[7];
  char* ws = (char*)d_ws;
  u16* xn = (u16*)ws; u16* y = (u16*)(ws + (4 << 20)); u16* qkv = (u16*)(ws + (8 << 20)); u16* h = (u16*)(ws + (24 << 20)); u16* o = (u16*)(ws + (40 << 20)); float* acc = (float*)(ws + (48 << 20));
  ln_kernel<<<S, 64, 0, stream>>>(x, xn, y);
  gemm_kernel<0><<<dim3(1920 / 64, S / 64), 256, 0, stream>>>(xn, wqkv, D, 1920, D, nullptr, qkv, 1920, nullptr);
  gemm_kernel<1><<<dim3(F / 64, S / 64), 256, 0, stream>>>(y, w1, D, F, D, b1, h, F, nullptr);
  attn_kernel<<<S * H / 256, 256, 0, stream>>>(qkv, o);
  gemm_kernel<2><<<dim3(D / 64, S / 64), 256, 0, stream>>>(o, wo, D, D, D, nullptr, nullptr, D, acc);
  gemm_kernel<3><<<dim3(D / 64, S / 64), 256, 0, stream>>>(h, w2, F, D, F, b2, nullptr, D, acc);
  final_kernel<<<S * D / 256, 256, 0, stream>>>(x, acc, (u16*)d_out);
}
```

```cpp
#include <hip/hip_runtime.h>
#include <stdint.h>
typedef unsigned short u16;
typedef unsigned long long u64;
typedef short s16x4 __attribute__((ext_vector_type(4)));
typedef short bf16x8 __attribute__((ext_vector_type(8)));
typedef float f32x16 __attribute__((ext_vector_type(16)));
typedef float f32x4 __attribute__((ext_vector_type(4)));
typedef float f32x2 __attribute__((ext_vector_type(2)));
typedef unsigned v4u __attribute__((ext_vector_type(4)));
typedef unsigned v2u __attribute__((ext_vector_type(2)));
typedef __bf16 bf2 __attribute__((ext_vector_type(2)));
#define LAS __attribute__((address_space(3)))
#define GAS __attribute__((address_space(1)))
#define DI __device__ __forceinline__
#define SB() __builtin_amdgcn_sched_barrier(0)
#define DMA(voff, sbase, ldsdst) asm volatile("s_mov_b32 m0, %2\n\ts_nop 3\n\tglobal_load_lds_dwordx4 %0, %1" :: "v"(voff), "s"(sbase), "s"(ldsdst) : "memory")
#define WBAR(n) asm volatile("s_waitcnt vmcnt(" #n ")\n\ts_barrier" ::: "memory")
#define WBARL(n) asm volatile("s_waitcnt vmcnt(" #n ") lgkmcnt(0)\n\ts_barrier" ::: "memory")
#define OPAQUE(x) asm volatile("" : "+v"(x))
#define MF32(a, b, c) __builtin_amdgcn_mfma_f32_32x32x16_bf16((a), (b), (c), 0, 0, 0)
#define MF16(a, b, c) __builtin_amdgcn_mfma_f32_16x16x32_bf16((a), (b), (c), 0, 0, 0)
DI float dot2(unsigned a, unsigned b, float c) { return __builtin_amdgcn_fdot2_f32_bf16(__builtin_bit_cast(bf2, a), __builtin_bit_cast(bf2, b), c, false); }
DI bf16x8 ldx(unsigned a) { return *(LAS bf16x8*)a; }
DI f32x4 ldf4(unsigned a) { return *(LAS f32x4*)a; }
DI f32x2 ldf2(unsigned a) { return *(LAS f32x2*)a; }
DI bf16x8 ldtr(unsigned a, unsigned a2) {
  s16x4 lo = __builtin_amdgcn_ds_read_tr16_b64_v4i16((LAS s16x4*)a);
  s16x4 hi = __builtin_amdgcn_ds_read_tr16_b64_v4i16((LAS s16x4*)a2);
  return __builtin_shufflevector(lo, hi, 0, 1, 2, 3, 4, 5, 6, 7);
}
DI float xhalf_sum(float s) { auto r = __builtin_amdgcn_permlane32_swap(__float_as_uint(s), __float_as_uint(s), false, false); return __uint_as_float(r[0]) + __uint_as_float(r[1]); }
#define DPP_ADD(x, ctrl, rm) x += __builtin_bit_cast(float, __builtin_amdgcn_update_dpp(0, __builtin_bit_cast(int, x), ctrl, rm, 0xf, false))
DI float wave_sum63(float x) { DPP_ADD(x, 0x111, 0xf); DPP_ADD(x, 0x112, 0xf); DPP_ADD(x, 0x114, 0xf); DPP_ADD(x, 0x118, 0xf); DPP_ADD(x, 0x142, 0xa); DPP_ADD(x, 0x143, 0xc); return x; }
DI float half_sum31(float x) { DPP_ADD(x, 0x111, 0xf); DPP_ADD(x, 0x112, 0xf); DPP_ADD(x, 0x114, 0xf); DPP_ADD(x, 0x118, 0xf); DPP_ADD(x, 0x142, 0xa); return x; }
DI float xrow_max(float x) { auto a = __builtin_amdgcn_permlane16_swap(__float_as_uint(x), __float_as_uint(x), false, false); x = fmaxf(__uint_as_float(a[0]), __uint_as_float(a[1]));
  auto b = __builtin_amdgcn_permlane32_swap(__float_as_uint(x), __float_as_uint(x), false, false); return fmaxf(__uint_as_float(b[0]), __uint_as_float(b[1])); }
DI float xrow_sum(float x) { auto a = __builtin_amdgcn_permlane16_swap(__float_as_uint(x), __float_as_uint(x), false, false); x = __uint_as_float(a[0]) + __uint_as_float(a[1]);
  auto b = __builtin_amdgcn_permlane32_swap(__float_as_uint(x), __float_as_uint(x), false, false); return __uint_as_float(b[0]) + __uint_as_float(b[1]); }
DI float bf2f(u16 v) { return __uint_as_float(((unsigned)v) << 16); }
#define CSUM2(s0, s1, a, b) { unsigned t0_, t1_, t2_, t3_; asm("v_lshlrev_b32 %2, 16, %6\n\tv_and_b32 %3, 0xffff0000, %6\n\tv_lshlrev_b32 %4, 16, %7\n\tv_and_b32 %5, 0xffff0000, %7\n\t" \
  "v_add_f32 %0, %0, %2\n\tv_add_f32 %1, %1, %3\n\tv_add_f32 %0, %0, %4\n\tv_add_f32 %1, %1, %5" : "+v"(s0), "+v"(s1), "=&v"(t0_), "=&v"(t1_), "=&v"(t2_), "=&v"(t3_) : "v"(a), "v"(b)); }
#define CSUM1(s0, s1, a) { unsigned t0_, t1_; asm("v_lshlrev_b32 %2, 16, %4\n\tv_and_b32 %3, 0xffff0000, %4\n\tv_add_f32 %0, %0, %2\n\tv_add_f32 %1, %1, %3" : "+v"(s0), "+v"(s1), "=&v"(t0_), "=&v"(t1_) : "v"(a)); }
#define I4_FMA_T(c, s, b) asm("v_fma_f32 %0, %4, %0, %5\n\tv_fma_f32 %1, %4, %1, %6\n\tv_fma_f32 %2, %4, %2, %7\n\tv_fma_f32 %3, %4, %3, %8" \
  : "+v"(c##0), "+v"(c##1), "+v"(c##2), "+v"(c##3) : "v"(s), "v"(b##0), "v"(b##1), "v"(b##2), "v"(b##3))
#define I4_MUL_S(c, s) asm("v_mul_f32 %0, %4, %0\n\tv_mul_f32 %1, %4, %1\n\tv_mul_f32 %2, %4, %2\n\tv_mul_f32 %3, %4, %3" \
  : "+v"(c##0), "+v"(c##1), "+v"(c##2), "+v"(c##3) : "v"(s))
#define I4_SQ(d, a) asm("v_mul_f32 %0, %4, %4\n\tv_mul_f32 %1, %5, %5\n\tv_mul_f32 %2, %6, %6\n\tv_mul_f32 %3, %7, %7" \
  : "=&v"(d##0), "=&v"(d##1), "=&v"(d##2), "=&v"(d##3) : "v"(a##0), "v"(a##1), "v"(a##2), "v"(a##3))
#define I4_MUL(d, a) asm("v_mul_f32 %0, %0, %4\n\tv_mul_f32 %1, %1, %5\n\tv_mul_f32 %2, %2, %6\n\tv_mul_f32 %3, %3, %7" \
  : "+v"(d##0), "+v"(d##1), "+v"(d##2), "+v"(d##3) : "v"(a##0), "v"(a##1), "v"(a##2), "v"(a##3))
#define I4_FMA_C(d, s1, s2) asm("v_fma_f32 %0, %0, %4, %5\n\tv_fma_f32 %1, %1, %4, %5\n\tv_fma_f32 %2, %2, %4, %5\n\tv_fma_f32 %3, %3, %4, %5" \
  : "+v"(d##0), "+v"(d##1), "+v"(d##2), "+v"(d##3) : "s"(s1), "v"(s2))
#define I4_EXP(d) asm("v_exp_f32 %0, %0\n\tv_exp_f32 %1, %1\n\tv_exp_f32 %2, %2\n\tv_exp_f32 %3, %3\n\ts_nop 0" : "+v"(d##0), "+v"(d##1), "+v"(d##2), "+v"(d##3))
#define I4_RCP(d) asm("v_rcp_f32 %0, %0\n\tv_rcp_f32 %1, %1\n\tv_rcp_f32 %2, %2\n\tv_rcp_f32 %3, %3\n\ts_nop 0" : "+v"(d##0), "+v"(d##1), "+v"(d##2), "+v"(d##3))
#define I4_ADD1(d) asm("v_add_f32 %0, 1.0, %0\n\tv_add_f32 %1, 1.0, %1\n\tv_add_f32 %2, 1.0, %2\n\tv_add_f32 %3, 1.0, %3" : "+v"(d##0), "+v"(d##1), "+v"(d##2), "+v"(d##3))
#define I2_PACK(p0, p1, a) asm("v_cvt_pk_bf16_f32 %0, %1, %2" : "=v"(p0) : "v"(a##0), "v"(a##1)); asm("v_cvt_pk_bf16_f32 %0, %1, %2" : "=v"(p1) : "v"(a##2), "v"(a##3))

#define WS_QKV_OFF (0)
#define WS_ACT_OFF (8u << 20)
#define WS_SYNC_OFF (20u << 20)
#define WS_STAT_OFF (WS_SYNC_OFF + 4096)
#define WS_FLAG1_OFF (WS_SYNC_OFF + 65536)
#define WS_FLAG2_OFF (WS_SYNC_OFF + 65536 + 4096)

#define RHO (pn & 7)
#define O_DIRECT 0
#define AKS 137u
#define CS_OFF 153600
#define ST_OFF 156160
#define TAG_OFF 158208
#define WARM_OFF 158464
#define B1_OFF 159488
#define IMG_OFF 81920
#define IMG_S 496
#define CODE_LINES 380
#define ROT0 0
#define KNEXT (kc == 0 ? 64 + RHO * 64 : (kc == 576 ? 64 : kc + 64))
DI v4u make_rsrc(const void* p, unsigned bytes) { u64 a = (u64)p; v4u r = {(unsigned)a, (unsigned)(a >> 32) & 0xffffu, bytes, 0x00020000u}; return r; }
#define BSTORE_SC1(v, rsrc, voff, soff) asm volatile("buffer_store_dwordx4 %0, %1, %2, %3 offen sc1\n\ts_nop 1" :: "v"(v), "v"(voff), "s"(rsrc), "s"(soff) : "memory")
DI float vmax(float a, float b) { return __builtin_amdgcn_fmed3f(a, b, 3.4028234663852886e38f); }
DI f32x4 row_bcast0(f32x4 v) { f32x4 r;
#pragma unroll
  for (int i = 0; i < 4; ++i) { float t = v[i]; r[i] = __builtin_bit_cast(float, __builtin_amdgcn_update_dpp(0, __builtin_bit_cast(int, t), 0x150, 0xf, 0xf, false)); }
  return r; }
DI unsigned pk2(f32x2 v) { return __builtin_bit_cast(unsigned, __builtin_convertvector(v, bf2)); }
DI v2u ln_pack(f32x4 acc, f32x4 cs, f32x2 ee) {
  const f32x2 ea = {ee[0], ee[0]}, eb = {ee[1], ee[1]};
  f32x2 a0 = {acc[0], acc[1]}, a1 = {acc[2], acc[3]}, c0 = {cs[0], cs[1]}, c1 = {cs[2], cs[3]};
  v2u r = {pk2(__builtin_elementwise_fma(a0, ea, c0 * eb)), pk2(__builtin_elementwise_fma(a1, ea, c1 * eb))};
  return r;
}
DI f32x2 gelu2(f32x2 v) {
  const float GC2 = -2.0f * 0.7978845608028654f * 1.4426950408889634f, GC1 = GC2 * 0.044715f;
  f32x2 u = v * v; u = __builtin_elementwise_fma(u, (f32x2){GC1, GC1}, (f32x2){GC2, GC2}); u = u * v;
  u[0] = __builtin_amdgcn_exp2f(u[0]); u[1] = __builtin_amdgcn_exp2f(u[1]);
  u = u + (f32x2){1.0f, 1.0f};
  u[0] = __builtin_amdgcn_rcpf(u[0]); u[1] = __builtin_amdgcn_rcpf(u[1]);
  return v * u;
}
DI v2u gelu_pack(f32x4 acc, f32x4 cs, f32x4 bb, f32x2 ee) {
  const f32x2 ea = {ee[0], ee[0]}, eb = {ee[1], ee[1]};
  f32x2 a0 = {acc[0], acc[1]}, a1 = {acc[2], acc[3]}, c0 = {cs[0], cs[1]}, c1 = {cs[2], cs[3]}, b0 = {bb[0], bb[1]}, b1 = {bb[2], bb[3]};
  v2u r = {pk2(gelu2(__builtin_elementwise_fma(a0, ea, __builtin_elementwise_fma(c0, eb, b0)))), pk2(gelu2(__builtin_elementwise_fma(a1, ea, __builtin_elementwise_fma(c1, eb, b1))))};
  return r;
}
DI void gelu_store32(const f32x16& acc, const f32x4 (&cv)[4], const f32x4 (&bv)[4], f32x2 ee, v4u orsrc, unsigned ovoff, unsigned soff) {
  v2u r[4];
#pragma unroll
  for (int q = 0; q < 4; ++q) { f32x4 a = {acc[4 * q], acc[4 * q + 1], acc[4 * q + 2], acc[4 * q + 3]}; r[q] = gelu_pack(a, cv[q], bv[q], ee); }
  v4u s0 = {r[0][0], r[0][1], r[1][0], r[1][1]}, s1 = {r[2][0], r[2][1], r[3][0], r[3][1]};
  BSTORE_SC1(s0, orsrc, ovoff, soff); BSTORE_SC1(s1, orsrc, ovoff, soff + 128);
}
#define POLL(fp, sel) { u64 t_; unsigned c_; asm volatile("s_cmp_lg_u32 %4, 0\n\ts_cbranch_scc1 2f\n\ts_mov_b32 %1, 0\n1:\n\tglobal_load_dwordx2 %0, %2, off sc1\n\ts_waitcnt vmcnt(0)\n\t" \
  "v_cmp_ne_u64 vcc, %3, %0\n\ts_add_u32 %1, %1, 1\n\ts_nop 2\n\ts_cbranch_vccz 2f\n\ts_cmp_gt_u32 %1, 0x40000\n\ts_cbranch_scc1 2f\n\ts_sleep 1\n\ts_branch 1b\n2:" \
  : "=&v"(t_), "=&s"(c_) : "v"(fp), "s"(tag), "s"(sel) : "memory", "vcc", "scc"); }
DI void attention(unsigned lb, int w, int pn, int hg, char* ws, u64 tag) {
  const int lane = threadIdx.x & 63, t16 = lane & 15, g = lane >> 4, h4 = w & 3;
  const unsigned colb = 40 * h4 + (g < 3 ? 16 * g : 0);
  const unsigned colb2 = colb + (g < 2 ? 8 : 0);
  const bool par = t16 & 1; const int sh = (t16 & 1) * 16;
  const bool ina = 4 * g + (t16 & 1) <= t16, inb = 4 * g + (t16 & 1) + 2 <= t16;
  const unsigned rb = lb + IMG_OFF + ((w >> 2) * 64 + t16) * IMG_S;
  const unsigned ra = rb + colb, ra2 = rb + colb2;
  const unsigned va = lb + IMG_OFF + ((w >> 2) * 64 + 4 * g + ((lane >> 2) & 3)) * IMG_S + 320 + 40 * h4 + (lane & 3) * 8;
  v2u q0[4], q1[4], k0[5], k1[5]; s16x4 vl[5], vh[5];
#pragma unroll
  for (int u = 0; u < 5; ++u) { k0[u] = *(LAS v2u*)(ra + 160 + u * 16 * IMG_S); k1[u] = *(LAS v2u*)(ra2 + 160 + u * 16 * IMG_S);
    if (u < 4) { q0[u] = *(LAS v2u*)(ra + (u + 1) * 16 * IMG_S); q1[u] = *(LAS v2u*)(ra2 + (u + 1) * 16 * IMG_S); } }
#pragma unroll
  for (int u = 0; u < 5; ++u) { vl[u] = __builtin_amdgcn_ds_read_tr16_b64_v4i16((LAS s16x4*)(va + u * 16 * IMG_S)); vh[u] = __builtin_amdgcn_ds_read_tr16_b64_v4i16((LAS s16x4*)(va + u * 16 * IMG_S + 32)); }
  f32x4 s0[4], s1[4];
#pragma unroll
  for (int u = 0; u < 4; ++u) {
    v4u qf = {g < 3 ? q0[u][0] : 0u, g < 3 ? q0[u][1] : 0u, g < 2 ? q1[u][0] : 0u, g < 2 ? q1[u][1] : 0u};
    v4u kf0 = {k0[u][0], k0[u][1], k1[u][0], k1[u][1]}, kf1 = {k0[u + 1][0], k0[u + 1][1], k1[u + 1][0], k1[u + 1][1]};
    s0[u] = MF16(__builtin_bit_cast(bf16x8, kf0), __builtin_bit_cast(bf16x8, qf), ((f32x4){0.f, 0.f, 0.f, 0.f}));
    s1[u] = MF16(__builtin_bit_cast(bf16x8, kf1), __builtin_bit_cast(bf16x8, qf), ((f32x4){0.f, 0.f, 0.f, 0.f}));
  }
  SB();
  float sa[4], sb[4], mx[4];
#pragma unroll
  for (int u = 0; u < 4; ++u) {
    sa[u] = ina ? (par ? s1[u][1] : s1[u][0]) : (par ? s0[u][1] : s0[u][0]);
    sb[u] = inb ? (par ? s1[u][3] : s1[u][2]) : (par ? s0[u][3] : s0[u][2]);
    if (u == 0) { const bool first = pn == 0 && w < 4; sa[u] = (ina || !first) ? sa[u] : -3.0e38f; sb[u] = (inb || !first) ? sb[u] : -3.0e38f; }
    mx[u] = vmax(sa[u], sb[u]);
  }
  {
    auto a = __builtin_amdgcn_permlane16_swap(__float_as_uint(mx[0]), __float_as_uint(mx[1]), false, false);
    auto b = __builtin_amdgcn_permlane16_swap(__float_as_uint(mx[2]), __float_as_uint(mx[3]), false, false);
    const float A = vmax(__uint_as_float(a[0]), __uint_as_float(a[1])), B = vmax(__uint_as_float(b[0]), __uint_as_float(b[1]));
    auto c = __builtin_amdgcn_permlane32_swap(__float_as_uint(A), __float_as_uint(B), false, false);
    const unsigned M = __float_as_uint(vmax(__uint_as_float(c[0]), __uint_as_float(c[1])));
    auto d = __builtin_amdgcn_permlane32_swap(M, M, false, false);
    unsigned d0 = d[0], d1 = d[1];
    auto e0 = __builtin_amdgcn_permlane16_swap(d0, d0, false, false); auto e1 = __builtin_amdgcn_permlane16_swap(d1, d1, false, false);
    mx[0] = __uint_as_float(e0[0]); mx[1] = __uint_as_float(e0[1]); mx[2] = __uint_as_float(e1[0]); mx[3] = __uint_as_float(e1[1]);
  }
  v4u pf[4];
#pragma unroll
  for (int u = 0; u < 4; ++u) {
    const float cexp = 0.22360679774997896f * 1.4426950408889634f;
    const unsigned pab = pk2((f32x2){__builtin_amdgcn_exp2f((sa[u] - mx[u]) * cexp), __builtin_amdgcn_exp2f((sb[u] - mx[u]) * cexp)});
    const unsigned pa = (pab & 0xffffu) << sh, pb = (pab >> 16) << sh;
    pf[u] = (v4u){ina ? 0u : pa, inb ? 0u : pb, ina ? pa : 0u, inb ? pb : 0u};
  }
  SB();
  const unsigned oa = rb + 40 * h4 + 8 * g, oa2 = rb + (g == 0 ? 40 * h4 + 32 : 480);
  const v4u ones = {0x3f803f80u, 0x3f803f80u, 0x3f803f80u, 0x3f803f80u};
  f32x4 o0[4], o1[4], dn[4];
#pragma unroll
  for (int u = 0; u < 4; ++u) {
    dn[u] = MF16(__builtin_bit_cast(bf16x8, ones), __builtin_bit_cast(bf16x8, pf[u]), ((f32x4){0.f, 0.f, 0.f, 0.f}));
    o0[u] = MF16(__builtin_shufflevector(vl[u], vl[u + 1], 0, 1, 2, 3, 4, 5, 6, 7), __builtin_bit_cast(bf16x8, pf[u]), ((f32x4){0.f, 0.f, 0.f, 0.f}));
    o1[u] = MF16(__builtin_shufflevector(vh[u], vh[u + 1], 0, 1, 2, 3, 4, 5, 6, 7), __builtin_bit_cast(bf16x8, pf[u]), ((f32x4){0.f, 0.f, 0.f, 0.f}));
  }
  SB();
#pragma unroll
  for (int u = 0; u < 4; ++u) {
    const float inv = __builtin_amdgcn_rcpf(dn[u][0]);
    { f32x2 a_ = {o0[u][0] * inv, o0[u][1] * inv}, b_ = {o0[u][2] * inv, o0[u][3] * inv}; v2u ov = {pk2(a_), pk2(b_)}; *(LAS v2u*)(oa + (u + 1) * 16 * IMG_S) = ov; }
    { f32x2 a_ = {o1[u][0] * inv, o1[u][1] * inv}, b_ = {o1[u][2] * inv, o1[u][3] * inv}; v2u ov = {pk2(a_), pk2(b_)}; *(LAS v2u*)(oa2 + (u + 1) * 16 * IMG_S) = ov; }
  }
  asm volatile("s_waitcnt lgkmcnt(0)\n\ts_barrier" ::: "memory");
}
DI void attention_out(unsigned lb, int pn, int hg, char* ws, u64 tag) {
  const v4u ors = make_rsrc(ws + WS_ACT_OFF, 512u * AKS * 128u);
  v4u v_[3]; unsigned go[3];
#pragma unroll
  for (int i = 0; i < 3; ++i) {
    const int c_ = i * 512 + (int)threadIdx.x; const int c = c_ < 1280 ? c_ : 0;
    const int line = c >> 3, quad = line / 5, unit = line - quad * 5, tq = (c >> 1) & 3, hf = c & 1;
    v_[i] = *(LAS v4u*)(lb + IMG_OFF + (16 + quad * 4 + tq) * IMG_S + unit * 32 + hf * 16);
    go[i] = c_ < 1280 ? ((32 * pn + quad) * AKS + 96 + hg * 5 + unit) * 128 + tq * 32 + hf * 16 : 0xfffffff0u;
  }
#pragma unroll
  for (int i = 0; i < 3; ++i) asm volatile("buffer_store_dwordx4 %0, %1, %2, 0 offen sc1\n\ts_nop 1" :: "v"(v_[i]), "v"(go[i]), "s"(ors) : "memory");
}
#define ATTENTION attention(lb, w, pn, ct, ws, tag);
#define ATTENTION2 attention_out(lb, pn, ct, ws, tag);

#define W3_SRC(i) ((i) < 12 ? w3h + (i) * (32 * 1280) : w3o + ((i) - 12) * (32 * 1280))
#define W3_LOAD(i, dst) { const char* s_ = W3_SRC(i); const unsigned d_ = lb + (dst) + cg * 4096; \
    DMA(w3off, s_, d_); DMA(w3off1, s_ + 8 * 1280, d_ + 1024); DMA(w3off, s_ + 16 * 1280, d_ + 2048); DMA(w3off1, s_ + 24 * 1280, d_ + 3072); }
#define W3_LOADH(i, dst) { const char* s_ = W3_SRC(i) + rp * (16 * 1280); const unsigned d_ = lb + (dst) + cg * 4096 + rp * 2048; \
    DMA(w3off, s_, d_); DMA(w3off1, s_ + 8 * 1280, d_ + 1024); }
#define DMA_MASK(voff, sbase, ldsdst, mask) asm volatile("s_mov_b32 m0, %2\n\ts_mov_b32 exec_lo, %3\n\ts_nop 2\n\tglobal_load_lds_dwordx4 %0, %1\n\ts_mov_b32 exec_lo, -1" :: "v"(voff), "s"(sbase), "s"(ldsdst), "s"(mask) : "memory")

#define TS_DECL
#define TS(i)
struct Tab { unsigned t[256]; };
#define TAGINC 0x9E3779B97F4A7C15ull
__global__ __launch_bounds__(512, 2) void fused_kernel(const u16* __restrict__ x, const u16* __restrict__ wqkv, const u16* __restrict__ wo, const u16* __restrict__ w1,
    const u16* __restrict__ b1, const u16* __restrict__ w2, const u16* __restrict__ b2, u16* __restrict__ out, char* __restrict__ ws, Tab tab) {
  __shared__ __attribute__((aligned(16))) char lds[163840];
  const unsigned lb = (unsigned)(size_t)(LAS char*)lds;
  const int lane = threadIdx.x & 63;
  const int w = __builtin_amdgcn_readfirstlane(threadIdx.x >> 6), cg = w & 3, rp = w >> 2;
  const unsigned tk = tab.t[blockIdx.x];
  u64 pc0; asm volatile("s_getpc_b64 %0" : "=s"(pc0));
  TS_DECL
  TS(0);
  asm volatile("" :: "s"(x), "s"(wqkv), "s"(wo), "s"(w1), "s"(b1), "s"(w2), "s"(b2), "s"(out), "s"(ws), "s"(tk));
  u64 tag = 0;
#define tag_lo ((unsigned)tag)
#define tag_hi ((unsigned)(tag >> 32))
  const char* statp = ws + WS_STAT_OFF;
#define ROW_LOAD2 v4u rowa, rowb; v2u rowc; asm volatile("s_load_dwordx2 %0, %1, 0x0" : "=s"(tag) : "s"(ws + WS_SYNC_OFF) : "memory"); \
  { const char* rp_ = (const char*)x + (size_t)(blockIdx.x * 8 + cg * 2) * 1280; unsigned o_ = lane * 40; \
    asm volatile("global_load_dwordx4 %0, %3, %4\n\tglobal_load_dwordx4 %1, %3, %4 offset:16\n\tglobal_load_dwordx2 %2, %3, %4 offset:32" : "=&v"(rowa), "=&v"(rowb), "=&v"(rowc) : "v"(o_), "s"(rp_) : "memory"); }
#define WARM { const char* p0_ = (const char*)(pc0 & ~127ull) + (size_t)((((blockIdx.x >> 3) & 7) * 64 + lane) % CODE_LINES) * 128; \
    const char* p1_ = lane < 3 ? (const char*)out + (size_t)lane * 1048576 : (const char*)b2; \
    const char* p2_ = (const char*)w2 + (size_t)(blockIdx.x * 60 + (lane < 60 ? lane : 0)) * 128; \
    const char* p3_ = (const char*)wo + (size_t)(blockIdx.x * 25 + (lane < 25 ? lane : 0)) * 128; \
    const char* p_ = cg == 0 ? p0_ : (cg == 1 ? p1_ : p1_); \
    asm volatile("s_mov_b32 m0, %1\n\ts_nop 0\n\tglobal_load_lds_dword %0, off" :: "v"(p_), "s"(lb + WARM_OFF + cg * 256) : "memory"); }
#define TOUCH { const int hh_ = pn >> 3, qq_ = ct >> 1, m_ = (pn & 7) * 2 + (ct & 1); \
    _Pragma("unroll") for (int n_ = 0; n_ < 4; ++n_) { int i_ = n_ * 4096 + (m_ * 4 + cg) * 64 + lane; i_ = i_ < 16000 ? i_ : 15999; \
      const int st_ = 2 + i_ / 2000, r_ = i_ % 2000; const char* p_; \
      if (r_ < 1040) p_ = (const char*)x + (size_t)max(1024 * hh_ - 16 + r_, 0) * 1280 + st_ * 128; \
      else if (r_ < 1616) { const int r2 = r_ - 1040, k_ = 64 * st_ + r2 / 9, j_ = r2 % 9; p_ = (const char*)wqkv + (size_t)k_ * 3840 + (j_ / 3) * 1280 + ((320 * qq_) & ~127) + (j_ % 3) * 128; } \
      else { const int r2 = r_ - 1616, k_ = 64 * st_ + r2 / 6; p_ = (const char*)w1 + (size_t)k_ * 3072 + 768 * qq_ + (r2 % 6) * 128; } \
      asm volatile("s_mov_b32 m0, %1\n\ts_nop 0\n\tglobal_load_lds_dword %0, off" :: "v"(p_), "s"(lb + WARM_OFF + cg * 256) : "memory"); } }
#define WARM2 { const int i_ = cg * 22 + (lane < 22 ? lane : 0); \
    const char* p_ = i_ < 60 ? (const char*)w2 + (size_t)(blockIdx.x * 60 + i_) * 128 : (const char*)wo + (size_t)(blockIdx.x * 25 + (i_ < 85 ? i_ - 60 : 0)) * 128; \
    asm volatile("s_mov_b32 m0, %1\n\ts_nop 0\n\tglobal_load_lds_dword %0, off" :: "v"(p_), "s"(lb + WARM_OFF + cg * 256) : "memory"); }
#define ACC2(u_) { unsigned a_ = (u_); s_ = dot2(a_, 0x3f803f80u, s_); q_ = dot2(a_, a_, q_); }
#define STATS_PRODUCE2(N) { asm volatile("s_waitcnt vmcnt(" #N ") lgkmcnt(0)" : "+v"(rowa), "+v"(rowb), "+v"(rowc), "+s"(tag) :: "memory"); tag += TAGINC; \
    float s_ = 0.f, q_ = 0.f; ACC2(rowa[0]) ACC2(rowa[1]) ACC2(rowa[2]) ACC2(rowa[3]) ACC2(rowb[0]) ACC2(rowb[1]) ACC2(rowb[2]) ACC2(rowb[3]) ACC2(rowc[0]) ACC2(rowc[1]) \
    s_ = half_sum31(s_); q_ = half_sum31(q_); \
    const float mean_ = s_ * (1.0f / 640.0f); const float var_ = fmaxf(__builtin_fmaf(-mean_, mean_, q_ * (1.0f / 640.0f)), 0.0f); \
    v4u g_ = {__float_as_uint(mean_), tag_lo, __float_as_uint(var_), tag_hi}; unsigned o_ = (blockIdx.x * 8 + cg * 2 + (lane >> 5)) * 16; \
    asm volatile("s_mov_b32 exec_lo, 0x80000000\n\ts_mov_b32 exec_hi, 0x80000000\n\tglobal_store_dwordx4 %0, %1, %2 sc1\n\ts_mov_b64 exec, -1\n\ts_nop 1" :: "v"(o_), "v"(g_), "s"(statp) : "memory"); \
    { v4u t_ = {tag_lo, tag_hi, 0u, 0u}; *(LAS v4u*)(lb + TAG_OFF) = t_; asm volatile("s_waitcnt lgkmcnt(0)" ::: "memory"); } }
#define TAG_FROM_LDS { v2u t_ = *(LAS v2u*)(lb + TAG_OFF); unsigned lo_ = t_[0], hi_ = t_[1]; tag = (u64)(unsigned)__builtin_amdgcn_readfirstlane(lo_) | ((u64)(unsigned)__builtin_amdgcn_readfirstlane(hi_) << 32); }
#define STATS_CONSUME(TYP) { \
    _Pragma("unroll 1") for (int sp_ = 0; sp_ < 0x40000 && !__all(stv[1] == tag_lo && stv[3] == tag_hi); ++sp_) { __builtin_amdgcn_s_sleep(2); \
      asm volatile("global_load_dwordx4 %0, %1, %2 sc1\n\ts_waitcnt vmcnt(0)" : "=&v"(stv) : "v"(stoff), "s"(statp) : "memory"); } \
    const float mean_ = __uint_as_float(stv[0]), var_ = __uint_as_float(stv[2]); \
    float ea_ = __builtin_amdgcn_rsqf(var_ + 1e-5f); \
    if (TYP) ea_ *= __builtin_amdgcn_rsqf(__builtin_fmaf(var_ * ea_, ea_, 1e-5f)); \
    f32x2 v_ = {ea_, -mean_ * ea_}; *(LAS f32x2*)(lb + ST_OFF + (threadIdx.x & 255) * 8) = v_; }
#define LATE_LANE(l_) int l_ = threadIdx.x; OPAQUE(l_);
  const int typ = (tk >> 1) & 1, ct = (tk >> 2) & 15, pn = (tk >> 6) & 15;
  const int t16 = lane & 15, g = lane >> 4;

  if (typ == 0 && rp == 1) {
  const unsigned xoff = (lane >> 3) * 1280 + (((lane & 7) ^ (lane >> 3)) << 4);
  const int rbase = pn * 128 - 16;
  unsigned woff; { const int par = lane >> 5, f_ = par | ((cg & 1) << 1) | ((cg >> 1) << 2); int s_ = ((lane >> 1) & 15) ^ f_; s_ = s_ == 15 ? 14 : s_;
    const int sec = s_ >= 10 ? 2 : (s_ >= 5 ? 1 : 0); woff = par * 3840 + sec * 1280 + (s_ - 5 * sec) * 32 + (lane & 1) * 16; }
  const char* wsrc = (const char*)wqkv + ct * 160 + (size_t)(2 * ((cg & 1) + 4 * (cg >> 1))) * 3840;
  const unsigned wd = lb + ((cg & 1) + 4 * (cg >> 1)) * 1024;
  const char* xs0 = (const char*)x + (size_t)max(rbase + 8 * cg, 0) * 1280;
  const char* xs1 = (const char*)x + (size_t)(rbase + 8 * cg + 32) * 1280;
  const int pl = cg < 2 ? cg + 16 : 17;
  const unsigned rmask = cg == 0 ? 0xffffff00u : 0xffffffffu;
  const char* xs4 = (const char*)x + (size_t)(rbase + 8 * pl) * 1280;
  unsigned so = 0; int kc = ROT0;
#define LOAD_A { const char* sw_ = wsrc + (size_t)kc * 3840; DMA_MASK(xoff, xs0 + kc * 2, lb + so + 32768 + cg * 1024, rmask); DMA(xoff, xs1 + kc * 2 + 0, lb + so + 36864 + cg * 1024); DMA(xoff, xs1 + kc * 2 + 40960, lb + so + 40960 + cg * 1024); DMA(xoff, xs1 + kc * 2 + 81920, lb + so + 45056 + cg * 1024); DMA(xoff, xs4 + kc * 2, lb + so + 32768 + pl * 1024); DMA(woff, sw_ + 0, wd + so + 0); DMA(woff, sw_ + 15360, wd + so + 2048); }
#define LOAD_B { const char* sw_ = wsrc + (size_t)kc * 3840; DMA(woff, sw_ + 61440, wd + so + 8192); DMA(woff, sw_ + 76800, wd + so + 10240); DMA(woff, sw_ + 122880, wd + so + 16384); DMA(woff, sw_ + 138240, wd + so + 18432); DMA(woff, sw_ + 184320, wd + so + 24576); DMA(woff, sw_ + 199680, wd + so + 26624); kc = KNEXT; so = so == 102400 ? 0 : so + 51200; }
  TS(14);
  LOAD_A LOAD_B
  TS(2);
  LOAD_A
  WBAR(7);
  TS(1);
  LOAD_B
  ROW_LOAD2
  LOAD_A
  WBAR(10);
  WARM2
  STATS_PRODUCE2(8);
#pragma unroll 1
  for (int it = 2; it < 9; ++it) {
    LOAD_B LOAD_A
    WBAR(7);
  }
  LOAD_B
#undef LOAD_A
#undef LOAD_B
  const unsigned stoff = min(max(rbase + (int)(threadIdx.x & 255), 0), 2047) * 16;
  v4u stv; asm volatile("global_load_dwordx4 %0, %1, %2 sc1" : "=v"(stv) : "v"(stoff), "s"(statp) : "memory");

  const int p3valid = (tk >> 10) & 1, ct3 = (tk >> 11) & 15;
  const unsigned w3off = (lane >> 3) * 1280 + ((((lane >> 1) & 3) ^ ((lane >> 4) & 1)) << 5) + (lane & 1) * 16;
  const unsigned w3off1 = w3off ^ 64;
  const char* w3h = (const char*)w2 + (size_t)(cg * 384) * 1280 + ct3 * 128;
  const char* w3o = (const char*)wo + (size_t)(cg * 160) * 1280 + ct3 * 128;

  W3_LOAD(0, 65536)
  WBAR(5);
  asm volatile("s_waitcnt vmcnt(4)" : "+v"(stv) :: "memory"); STATS_CONSUME(0);
  asm volatile("s_waitcnt lgkmcnt(0)\n\ts_barrier" ::: "memory");
  asm volatile("s_barrier" ::: "memory");
  W3_LOAD(1, 0) W3_LOAD(2, 16384) W3_LOAD(3, 32768) W3_LOAD(4, 49152)
  } else if (typ == 0 && cg < 2) {
  const int th = cg & 1;
  WARM
  unsigned xa0 = lb + 32768 + t16 * 128 + ((g ^ (t16 & 7)) << 4), xa1 = xa0 ^ 64;
  const unsigned xth = th * 8192;
  unsigned xh0 = xa0, xh1 = xa1; xa0 += xth + 2048; xa1 += xth + 2048;
  const unsigned wb_ = lb + (8 * g + ((lane >> 2) & 3)) * 512 + (lane & 3) * 8, wf_ = ((lane >> 2) & 3) | ((g & 1) << 2);
  const int cb0 = th ? 4 : 0; unsigned wa0 = wb_ + ((cb0 ^ wf_) << 5);
  const int cb1 = th ? 5 : 1; unsigned wa1 = wb_ + ((cb1 ^ wf_) << 5);
  const int cb2 = th ? 6 : 2; unsigned wa2 = wb_ + ((cb2 ^ wf_) << 5);
  const int cb3 = th ? 7 : 3; unsigned wa3 = wb_ + ((cb3 ^ wf_) << 5);
  const int cb4 = th ? 0 : 4; unsigned wa4 = wb_ + ((cb4 ^ wf_) << 5);
  const int cb5 = th ? 1 : 5; unsigned wa5 = wb_ + ((cb5 ^ wf_) << 5);
  const int cb6 = th ? 2 : 6; unsigned wa6 = wb_ + ((cb6 ^ wf_) << 5);
  const int cb7 = th ? 3 : 7; unsigned wa7 = wb_ + ((cb7 ^ wf_) << 5);
  if (cg == 0 && lane < 24) { v4u one_ = {0x3f803f80u, 0x3f803f80u, 0x3f803f80u, 0x3f803f80u}; *(LAS v4u*)(lb + (lane >> 3) * 51200 + 32768 + (lane & 7) * 16) = one_; }
  f32x4 acc0_0 = {0.f, 0.f, 0.f, 0.f}; f32x4 acc0_1 = {0.f, 0.f, 0.f, 0.f}; f32x4 acc0_2 = {0.f, 0.f, 0.f, 0.f}; f32x4 acc0_3 = {0.f, 0.f, 0.f, 0.f}; f32x4 acc0_4 = {0.f, 0.f, 0.f, 0.f}; f32x4 acc0_5 = {0.f, 0.f, 0.f, 0.f}; f32x4 acc0_6 = {0.f, 0.f, 0.f, 0.f}; f32x4 acc0_7 = {0.f, 0.f, 0.f, 0.f}; f32x4 acc1_0 = {0.f, 0.f, 0.f, 0.f}; f32x4 acc1_1 = {0.f, 0.f, 0.f, 0.f}; f32x4 acc1_2 = {0.f, 0.f, 0.f, 0.f}; f32x4 acc1_3 = {0.f, 0.f, 0.f, 0.f}; f32x4 acc1_4 = {0.f, 0.f, 0.f, 0.f}; f32x4 acc1_5 = {0.f, 0.f, 0.f, 0.f}; f32x4 acc1_6 = {0.f, 0.f, 0.f, 0.f}; f32x4 acc1_7 = {0.f, 0.f, 0.f, 0.f}; f32x4 acc2_0 = {0.f, 0.f, 0.f, 0.f}; f32x4 acc2_1 = {0.f, 0.f, 0.f, 0.f}; f32x4 acc2_2 = {0.f, 0.f, 0.f, 0.f}; f32x4 acc2_3 = {0.f, 0.f, 0.f, 0.f}; f32x4 acc2_4 = {0.f, 0.f, 0.f, 0.f}; f32x4 acc2_5 = {0.f, 0.f, 0.f, 0.f}; f32x4 acc2_6 = {0.f, 0.f, 0.f, 0.f}; f32x4 acc2_7 = {0.f, 0.f, 0.f, 0.f}; f32x4 acc3_0 = {0.f, 0.f, 0.f, 0.f}; f32x4 acc3_1 = {0.f, 0.f, 0.f, 0.f}; f32x4 acc3_2 = {0.f, 0.f, 0.f, 0.f}; f32x4 acc3_3 = {0.f, 0.f, 0.f, 0.f}; f32x4 acc3_4 = {0.f, 0.f, 0.f, 0.f}; f32x4 acc3_5 = {0.f, 0.f, 0.f, 0.f}; f32x4 acc3_6 = {0.f, 0.f, 0.f, 0.f}; f32x4 acc3_7 = {0.f, 0.f, 0.f, 0.f};
  f32x4 acch_0 = {0.f, 0.f, 0.f, 0.f}; f32x4 acch_1 = {0.f, 0.f, 0.f, 0.f}; f32x4 acch_2 = {0.f, 0.f, 0.f, 0.f}; f32x4 acch_3 = {0.f, 0.f, 0.f, 0.f};
  TS(9);
  asm volatile("s_waitcnt lgkmcnt(0)\n\ts_barrier" ::: "memory"); SB();
  TS(1);
  bf16x8 xA0, xB0, xA1, xB1, xA2, xB2, xA3, xB3, xA4, xB4;
  bf16x8 wc0 = ldtr(wa0 + 0, wa0 + 2048); SB();
  xA0 = ldx(xa0 + 0); SB();
  xA1 = ldx(xa0 + 2048); SB();
  bf16x8 wc1 = ldtr(wa1 + 0, wa1 + 2048); SB();
  xA2 = ldx(xa0 + 4096); SB();
  xA3 = ldx(xa0 + 6144); SB();
  bf16x8 wc2 = ldtr(wa2 + 0, wa2 + 2048); SB();
  xA4 = ldx(xh0); SB();
  bf16x8 wc3 = ldtr(wa3 + 0, wa3 + 2048); SB();
  SB();
  int sl = 0;
#pragma unroll 1
  for (int j = 0; j < 10; ++j) {
    acc0_0 = MF16(wc0, xA0, acc0_0);
    SB();
    acc1_0 = MF16(wc0, xA1, acc1_0);
    SB();
    bf16x8 wf4 = ldtr(wa4 + 0, wa4 + 2048);
    acc2_0 = MF16(wc0, xA2, acc2_0);
    SB();
    acc3_0 = MF16(wc0, xA3, acc3_0);
    SB();
    acch_0 = MF16(wc0, xA4, acch_0);
    SB();
    acc0_1 = MF16(wc1, xA0, acc0_1);
    SB();
    acc1_1 = MF16(wc1, xA1, acc1_1);
    SB();
    bf16x8 wf5 = ldtr(wa5 + 0, wa5 + 2048);
    acc2_1 = MF16(wc1, xA2, acc2_1);
    SB();
    xB0 = ldx(xa1 + 0);
    acc3_1 = MF16(wc1, xA3, acc3_1);
    SB();
    acch_1 = MF16(wc1, xA4, acch_1);
    SB();
    acc0_2 = MF16(wc2, xA0, acc0_2);
    SB();
    acc1_2 = MF16(wc2, xA1, acc1_2);
    SB();
    bf16x8 wf6 = ldtr(wa6 + 0, wa6 + 2048);
    acc2_2 = MF16(wc2, xA2, acc2_2);
    SB();
    xB1 = ldx(xa1 + 2048);
    acc3_2 = MF16(wc2, xA3, acc3_2);
    SB();
    acch_2 = MF16(wc2, xA4, acch_2);
    SB();
    acc0_3 = MF16(wc3, xA0, acc0_3);
    SB();
    acc1_3 = MF16(wc3, xA1, acc1_3);
    SB();
    bf16x8 wf7 = ldtr(wa7 + 0, wa7 + 2048);
    acc2_3 = MF16(wc3, xA2, acc2_3);
    SB();
    xB2 = ldx(xa1 + 4096);
    acc3_3 = MF16(wc3, xA3, acc3_3);
    SB();
    acch_3 = MF16(wc3, xA4, acch_3);
    SB();
    acc0_4 = MF16(wf4, xA0, acc0_4);
    SB();
    acc1_4 = MF16(wf4, xA1, acc1_4);
    SB();
    bf16x8 wf8 = ldtr(wa0 + 16384, wa0 + 18432);
    acc2_4 = MF16(wf4, xA2, acc2_4);
    SB();
    xB3 = ldx(xa1 + 6144);
    acc3_4 = MF16(wf4, xA3, acc3_4);
    SB();
    acc0_5 = MF16(wf5, xA0, acc0_5);
    SB();
    acc1_5 = MF16(wf5, xA1, acc1_5);
    SB();
    bf16x8 wf9 = ldtr(wa1 + 16384, wa1 + 18432);
    acc2_5 = MF16(wf5, xA2, acc2_5);
    SB();
    xB4 = ldx(xh1);
    acc3_5 = MF16(wf5, xA3, acc3_5);
    SB();
    acc0_6 = MF16(wf6, xA0, acc0_6);
    SB();
    acc1_6 = MF16(wf6, xA1, acc1_6);
    SB();
    bf16x8 wf10 = ldtr(wa2 + 16384, wa2 + 18432);
    acc2_6 = MF16(wf6, xA2, acc2_6);
    SB();
    acc3_6 = MF16(wf6, xA3, acc3_6);
    SB();
    acc0_7 = MF16(wf7, xA0, acc0_7);
    SB();
    acc1_7 = MF16(wf7, xA1, acc1_7);
    SB();
    bf16x8 wf11 = ldtr(wa3 + 16384, wa3 + 18432);
    acc2_7 = MF16(wf7, xA2, acc2_7);
    SB();
    acc3_7 = MF16(wf7, xA3, acc3_7);
    SB();
    acc0_0 = MF16(wf8, xB0, acc0_0);
    SB();
    acc1_0 = MF16(wf8, xB1, acc1_0);
    SB();
    bf16x8 wf12 = ldtr(wa4 + 16384, wa4 + 18432);
    acc2_0 = MF16(wf8, xB2, acc2_0);
    SB();
    acc3_0 = MF16(wf8, xB3, acc3_0);
    SB();
    acch_0 = MF16(wf8, xB4, acch_0);
    SB();
    acc0_1 = MF16(wf9, xB0, acc0_1);
    SB();
    acc1_1 = MF16(wf9, xB1, acc1_1);
    SB();
    bf16x8 wf13 = ldtr(wa5 + 16384, wa5 + 18432);
    acc2_1 = MF16(wf9, xB2, acc2_1);
    SB();
    acc3_1 = MF16(wf9, xB3, acc3_1);
    SB();
    acch_1 = MF16(wf9, xB4, acch_1);
    SB();
    acc0_2 = MF16(wf10, xB0, acc0_2);
    SB();
    acc1_2 = MF16(wf10, xB1, acc1_2);
    SB();
    bf16x8 wf14 = ldtr(wa6 + 16384, wa6 + 18432);
    acc2_2 = MF16(wf10, xB2, acc2_2);
    SB();
    acc3_2 = MF16(wf10, xB3, acc3_2);
    SB();
    acch_2 = MF16(wf10, xB4, acch_2);
    SB();
    acc0_3 = MF16(wf11, xB0, acc0_3);
    SB();
    acc1_3 = MF16(wf11, xB1, acc1_3);
    SB();
    bf16x8 wf15 = ldtr(wa7 + 16384, wa7 + 18432);
    acc2_3 = MF16(wf11, xB2, acc2_3);
    SB();
    acc3_3 = MF16(wf11, xB3, acc3_3);
    SB();
    acch_3 = MF16(wf11, xB4, acch_3);
    SB();
    acc0_4 = MF16(wf12, xB0, acc0_4);
    SB();
    acc1_4 = MF16(wf12, xB1, acc1_4);
    SB();
    asm volatile("s_waitcnt lgkmcnt(0)\n\ts_barrier" ::: "memory"); SB();
    const int d_ = sl == 2 ? -102400 : 51200; sl = sl == 2 ? 0 : sl + 1; xa0 += d_; xh0 += d_;
    wa0 += d_; bf16x8 wf16 = ldtr(wa0 + 0, wa0 + 2048);
    acc2_4 = MF16(wf12, xB2, acc2_4);
    SB();
    xA0 = ldx(xa0 + 0); xA1 = ldx(xa0 + 2048);
    acc3_4 = MF16(wf12, xB3, acc3_4);
    SB();
    xa1 += d_; xh1 += d_;
    acc0_5 = MF16(wf13, xB0, acc0_5);
    SB();
    wa4 += d_;
    acc1_5 = MF16(wf13, xB1, acc1_5);
    SB();
    wa1 += d_; bf16x8 wf17 = ldtr(wa1 + 0, wa1 + 2048);
    acc2_5 = MF16(wf13, xB2, acc2_5);
    SB();
    xA2 = ldx(xa0 + 4096); xA3 = ldx(xa0 + 6144);
    acc3_5 = MF16(wf13, xB3, acc3_5);
    SB();
    wa5 += d_;
    acc0_6 = MF16(wf14, xB0, acc0_6);
    SB();
    wa6 += d_;
    acc1_6 = MF16(wf14, xB1, acc1_6);
    SB();
    wa2 += d_; bf16x8 wf18 = ldtr(wa2 + 0, wa2 + 2048);
    acc2_6 = MF16(wf14, xB2, acc2_6);
    SB();
    xA4 = ldx(xh0);
    acc3_6 = MF16(wf14, xB3, acc3_6);
    SB();
    wa7 += d_;
    acc0_7 = MF16(wf15, xB0, acc0_7);
    SB();
    acc1_7 = MF16(wf15, xB1, acc1_7);
    SB();
    wa3 += d_; bf16x8 wf19 = ldtr(wa3 + 0, wa3 + 2048);
    acc2_7 = MF16(wf15, xB2, acc2_7);
    SB();
    acc3_7 = MF16(wf15, xB3, acc3_7);
    SB();
    wc0 = wf16; wc1 = wf17; wc2 = wf18; wc3 = wf19;
  }
  TS(3);
  TAG_FROM_LDS
  LATE_LANE(l_)
  if ((l_ & 15) == 0) *(LAS f32x4*)(lb + CS_OFF + cb0 * 64 + ((l_ >> 4) & 3) * 16) = acch_0;
  if ((l_ & 15) == 0) *(LAS f32x4*)(lb + CS_OFF + cb1 * 64 + ((l_ >> 4) & 3) * 16) = acch_1;
  if ((l_ & 15) == 0) *(LAS f32x4*)(lb + CS_OFF + cb2 * 64 + ((l_ >> 4) & 3) * 16) = acch_2;
  if ((l_ & 15) == 0) *(LAS f32x4*)(lb + CS_OFF + cb3 * 64 + ((l_ >> 4) & 3) * 16) = acch_3;
  TS(2);
  { const unsigned sta = lb + ST_OFF + (l_ & 15) * 8, csa = lb + CS_OFF + ((l_ >> 4) & 3) * 16;
    const unsigned ia = lb + 81920 + (l_ & 15) * 496 + ((l_ >> 4) & 3) * 8;
    const f32x2 eh = ldf2(sta); const f32x2 e0 = ldf2(sta + (1 + 4 * th + 0) * 128); const f32x2 e1 = ldf2(sta + (1 + 4 * th + 1) * 128); const f32x2 e2 = ldf2(sta + (1 + 4 * th + 2) * 128); const f32x2 e3 = ldf2(sta + (1 + 4 * th + 3) * 128);
    const unsigned ib = ia + (1 + 4 * th) * 7936;
    asm volatile("s_waitcnt lgkmcnt(0)\n\ts_barrier" ::: "memory"); SB();
    const f32x4 cv0 = ldf4(csa + cb0 * 64); const f32x4 cv1 = ldf4(csa + cb1 * 64); const f32x4 cv2 = ldf4(csa + cb2 * 64); const f32x4 cv3 = ldf4(csa + cb3 * 64); const f32x4 cv4 = ldf4(csa + cb4 * 64); const f32x4 cv5 = ldf4(csa + cb5 * 64); const f32x4 cv6 = ldf4(csa + cb6 * 64); const f32x4 cv7 = ldf4(csa + cb7 * 64);
    { const f32x4 c_ = cv0; const unsigned a_ = ia + cb0 * 32, b_ = ib + cb0 * 32;
      *(LAS v2u*)(b_ + 0) = ln_pack(acc0_0, c_, e0);
      *(LAS v2u*)(b_ + 7936) = ln_pack(acc1_0, c_, e1);
      *(LAS v2u*)(b_ + 15872) = ln_pack(acc2_0, c_, e2);
      *(LAS v2u*)(b_ + 23808) = ln_pack(acc3_0, c_, e3);
      *(LAS v2u*)(a_) = ln_pack(acch_0, c_, eh);
    }
    { const f32x4 c_ = cv1; const unsigned a_ = ia + cb1 * 32, b_ = ib + cb1 * 32;
      *(LAS v2u*)(b_ + 0) = ln_pack(acc0_1, c_, e0);
      *(LAS v2u*)(b_ + 7936) = ln_pack(acc1_1, c_, e1);
      *(LAS v2u*)(b_ + 15872) = ln_pack(acc2_1, c_, e2);
      *(LAS v2u*)(b_ + 23808) = ln_pack(acc3_1, c_, e3);
      *(LAS v2u*)(a_) = ln_pack(acch_1, c_, eh);
    }
    { const f32x4 c_ = cv2; const unsigned a_ = ia + cb2 * 32, b_ = ib + cb2 * 32;
      *(LAS v2u*)(b_ + 0) = ln_pack(acc0_2, c_, e0);
      *(LAS v2u*)(b_ + 7936) = ln_pack(acc1_2, c_, e1);
      *(LAS v2u*)(b_ + 15872) = ln_pack(acc2_2, c_, e2);
      *(LAS v2u*)(b_ + 23808) = ln_pack(acc3_2, c_, e3);
      *(LAS v2u*)(a_) = ln_pack(acch_2, c_, eh);
    }
    { const f32x4 c_ = cv3; const unsigned a_ = ia + cb3 * 32, b_ = ib + cb3 * 32;
      *(LAS v2u*)(b_ + 0) = ln_pack(acc0_3, c_, e0);
      *(LAS v2u*)(b_ + 7936) = ln_pack(acc1_3, c_, e1);
      *(LAS v2u*)(b_ + 15872) = ln_pack(acc2_3, c_, e2);
      *(LAS v2u*)(b_ + 23808) = ln_pack(acc3_3, c_, e3);
      *(LAS v2u*)(a_) = ln_pack(acch_3, c_, eh);
    }
    { const f32x4 c_ = cv4; const unsigned a_ = ia + cb4 * 32, b_ = ib + cb4 * 32;
      *(LAS v2u*)(b_ + 0) = ln_pack(acc0_4, c_, e0);
      *(LAS v2u*)(b_ + 7936) = ln_pack(acc1_4, c_, e1);
      *(LAS v2u*)(b_ + 15872) = ln_pack(acc2_4, c_, e2);
      *(LAS v2u*)(b_ + 23808) = ln_pack(acc3_4, c_, e3);
    }
    { const f32x4 c_ = cv5; const unsigned a_ = ia + cb5 * 32, b_ = ib + cb5 * 32;
      *(LAS v2u*)(b_ + 0) = ln_pack(acc0_5, c_, e0);
      *(LAS v2u*)(b_ + 7936) = ln_pack(acc1_5, c_, e1);
      *(LAS v2u*)(b_ + 15872) = ln_pack(acc2_5, c_, e2);
      *(LAS v2u*)(b_ + 23808) = ln_pack(acc3_5, c_, e3);
    }
    { const f32x4 c_ = cv6; const unsigned a_ = ia + cb6 * 32, b_ = ib + cb6 * 32;
      *(LAS v2u*)(b_ + 0) = ln_pack(acc0_6, c_, e0);
      *(LAS v2u*)(b_ + 7936) = ln_pack(acc1_6, c_, e1);
      *(LAS v2u*)(b_ + 15872) = ln_pack(acc2_6, c_, e2);
      *(LAS v2u*)(b_ + 23808) = ln_pack(acc3_6, c_, e3);
    }
    { const f32x4 c_ = cv7; const unsigned a_ = ia + cb7 * 32, b_ = ib + cb7 * 32;
      *(LAS v2u*)(b_ + 0) = ln_pack(acc0_7, c_, e0);
      *(LAS v2u*)(b_ + 7936) = ln_pack(acc1_7, c_, e1);
      *(LAS v2u*)(b_ + 15872) = ln_pack(acc2_7, c_, e2);
      *(LAS v2u*)(b_ + 23808) = ln_pack(acc3_7, c_, e3);
    }
  }
  TS(4);
  } else if (typ == 0) {
  const int th = cg & 1;
  WARM
  unsigned xa0 = lb + 32768 + t16 * 128 + ((g ^ (t16 & 7)) << 4), xa1 = xa0 ^ 64;
  const unsigned xth = th * 8192;
  unsigned xh0 = xa0, xh1 = xa1; xa0 += xth + 2048; xa1 += xth + 2048;
  const unsigned wb_ = lb + (8 * g + ((lane >> 2) & 3)) * 512 + (lane & 3) * 8, wf_ = ((lane >> 2) & 3) | ((g & 1) << 2);
  const int cb0 = th ? 11 : 8; unsigned wa0 = wb_ + ((cb0 ^ wf_) << 5);
  const int cb1 = th ? 12 : 9; unsigned wa1 = wb_ + ((cb1 ^ wf_) << 5);
  const int cb2 = th ? 13 : 10; unsigned wa2 = wb_ + ((cb2 ^ wf_) << 5);
  const int cb3 = th ? 14 : 11; unsigned wa3 = wb_ + ((cb3 ^ wf_) << 5);
  const int cb4 = th ? 8 : 12; unsigned wa4 = wb_ + ((cb4 ^ wf_) << 5);
  const int cb5 = th ? 9 : 13; unsigned wa5 = wb_ + ((cb5 ^ wf_) << 5);
  const int cb6 = th ? 10 : 14; unsigned wa6 = wb_ + ((cb6 ^ wf_) << 5);
  if (cg == 0 && lane < 24) { v4u one_ = {0x3f803f80u, 0x3f803f80u, 0x3f803f80u, 0x3f803f80u}; *(LAS v4u*)(lb + (lane >> 3) * 51200 + 32768 + (lane & 7) * 16) = one_; }
  f32x4 acc0_0 = {0.f, 0.f, 0.f, 0.f}; f32x4 acc0_1 = {0.f, 0.f, 0.f, 0.f}; f32x4 acc0_2 = {0.f, 0.f, 0.f, 0.f}; f32x4 acc0_3 = {0.f, 0.f, 0.f, 0.f}; f32x4 acc0_4 = {0.f, 0.f, 0.f, 0.f}; f32x4 acc0_5 = {0.f, 0.f, 0.f, 0.f}; f32x4 acc0_6 = {0.f, 0.f, 0.f, 0.f}; f32x4 acc1_0 = {0.f, 0.f, 0.f, 0.f}; f32x4 acc1_1 = {0.f, 0.f, 0.f, 0.f}; f32x4 acc1_2 = {0.f, 0.f, 0.f, 0.f}; f32x4 acc1_3 = {0.f, 0.f, 0.f, 0.f}; f32x4 acc1_4 = {0.f, 0.f, 0.f, 0.f}; f32x4 acc1_5 = {0.f, 0.f, 0.f, 0.f}; f32x4 acc1_6 = {0.f, 0.f, 0.f, 0.f}; f32x4 acc2_0 = {0.f, 0.f, 0.f, 0.f}; f32x4 acc2_1 = {0.f, 0.f, 0.f, 0.f}; f32x4 acc2_2 = {0.f, 0.f, 0.f, 0.f}; f32x4 acc2_3 = {0.f, 0.f, 0.f, 0.f}; f32x4 acc2_4 = {0.f, 0.f, 0.f, 0.f}; f32x4 acc2_5 = {0.f, 0.f, 0.f, 0.f}; f32x4 acc2_6 = {0.f, 0.f, 0.f, 0.f}; f32x4 acc3_0 = {0.f, 0.f, 0.f, 0.f}; f32x4 acc3_1 = {0.f, 0.f, 0.f, 0.f}; f32x4 acc3_2 = {0.f, 0.f, 0.f, 0.f}; f32x4 acc3_3 = {0.f, 0.f, 0.f, 0.f}; f32x4 acc3_4 = {0.f, 0.f, 0.f, 0.f}; f32x4 acc3_5 = {0.f, 0.f, 0.f, 0.f}; f32x4 acc3_6 = {0.f, 0.f, 0.f, 0.f};
  f32x4 acch_0 = {0.f, 0.f, 0.f, 0.f}; f32x4 acch_1 = {0.f, 0.f, 0.f, 0.f}; f32x4 acch_2 = {0.f, 0.f, 0.f, 0.f}; f32x4 acch_3 = {0.f, 0.f, 0.f, 0.f};
  TS(9);
  asm volatile("s_waitcnt lgkmcnt(0)\n\ts_barrier" ::: "memory"); SB();
  TS(1);
  bf16x8 xA0, xB0, xA1, xB1, xA2, xB2, xA3, xB3, xA4, xB4;
  bf16x8 wc0 = ldtr(wa0 + 0, wa0 + 2048); SB();
  xA0 = ldx(xa0 + 0); SB();
  xA1 = ldx(xa0 + 2048); SB();
  bf16x8 wc1 = ldtr(wa1 + 0, wa1 + 2048); SB();
  xA2 = ldx(xa0 + 4096); SB();
  xA3 = ldx(xa0 + 6144); SB();
  bf16x8 wc2 = ldtr(wa2 + 0, wa2 + 2048); SB();
  xA4 = ldx(xh0); SB();
  bf16x8 wc3 = ldtr(wa3 + 0, wa3 + 2048); SB();
  SB();
  int sl = 0;
#pragma unroll 1
  for (int j = 0; j < 10; ++j) {
    acc0_0 = MF16(wc0, xA0, acc0_0);
    SB();
    acc1_0 = MF16(wc0, xA1, acc1_0);
    SB();
    bf16x8 wf4 = ldtr(wa4 + 0, wa4 + 2048);
    acc2_0 = MF16(wc0, xA2, acc2_0);
    SB();
    acc3_0 = MF16(wc0, xA3, acc3_0);
    SB();
    acch_0 = MF16(wc0, xA4, acch_0);
    SB();
    acc0_1 = MF16(wc1, xA0, acc0_1);
    SB();
    acc1_1 = MF16(wc1, xA1, acc1_1);
    SB();
    bf16x8 wf5 = ldtr(wa5 + 0, wa5 + 2048);
    acc2_1 = MF16(wc1, xA2, acc2_1);
    SB();
    xB0 = ldx(xa1 + 0);
    acc3_1 = MF16(wc1, xA3, acc3_1);
    SB();
    acch_1 = MF16(wc1, xA4, acch_1);
    SB();
    acc0_2 = MF16(wc2, xA0, acc0_2);
    SB();
    acc1_2 = MF16(wc2, xA1, acc1_2);
    SB();
    bf16x8 wf6 = ldtr(wa6 + 0, wa6 + 2048);
    acc2_2 = MF16(wc2, xA2, acc2_2);
    SB();
    xB1 = ldx(xa1 + 2048);
    acc3_2 = MF16(wc2, xA3, acc3_2);
    SB();
    acch_2 = MF16(wc2, xA4, acch_2);
    SB();
    acc0_3 = MF16(wc3, xA0, acc0_3);
    SB();
    acc1_3 = MF16(wc3, xA1, acc1_3);
    SB();
    bf16x8 wf7 = ldtr(wa0 + 16384, wa0 + 18432);
    acc2_3 = MF16(wc3, xA2, acc2_3);
    SB();
    xB2 = ldx(xa1 + 4096);
    acc3_3 = MF16(wc3, xA3, acc3_3);
    SB();
    acch_3 = MF16(wc3, xA4, acch_3);
    SB();
    acc0_4 = MF16(wf4, xA0, acc0_4);
    SB();
    acc1_4 = MF16(wf4, xA1, acc1_4);
    SB();
    bf16x8 wf8 = ldtr(wa1 + 16384, wa1 + 18432);
    acc2_4 = MF16(wf4, xA2, acc2_4);
    SB();
    xB3 = ldx(xa1 + 6144);
    acc3_4 = MF16(wf4, xA3, acc3_4);
    SB();
    acc0_5 = MF16(wf5, xA0, acc0_5);
    SB();
    acc1_5 = MF16(wf5, xA1, acc1_5);
    SB();
    bf16x8 wf9 = ldtr(wa2 + 16384, wa2 + 18432);
    acc2_5 = MF16(wf5, xA2, acc2_5);
    SB();
    xB4 = ldx(xh1);
    acc3_5 = MF16(wf5, xA3, acc3_5);
    SB();
    acc0_6 = MF16(wf6, xA0, acc0_6);
    SB();
    acc1_6 = MF16(wf6, xA1, acc1_6);
    SB();
    bf16x8 wf10 = ldtr(wa3 + 16384, wa3 + 18432);
    acc2_6 = MF16(wf6, xA2, acc2_6);
    SB();
    acc3_6 = MF16(wf6, xA3, acc3_6);
    SB();
    acc0_0 = MF16(wf7, xB0, acc0_0);
    SB();
    acc1_0 = MF16(wf7, xB1, acc1_0);
    SB();
    bf16x8 wf11 = ldtr(wa4 + 16384, wa4 + 18432);
    acc2_0 = MF16(wf7, xB2, acc2_0);
    SB();
    acc3_0 = MF16(wf7, xB3, acc3_0);
    SB();
    acch_0 = MF16(wf7, xB4, acch_0);
    SB();
    acc0_1 = MF16(wf8, xB0, acc0_1);
    SB();
    acc1_1 = MF16(wf8, xB1, acc1_1);
    SB();
    bf16x8 wf12 = ldtr(wa5 + 16384, wa5 + 18432);
    acc2_1 = MF16(wf8, xB2, acc2_1);
    SB();
    acc3_1 = MF16(wf8, xB3, acc3_1);
    SB();
    acch_1 = MF16(wf8, xB4, acch_1);
    SB();
    acc0_2 = MF16(wf9, xB0, acc0_2);
    SB();
    acc1_2 = MF16(wf9, xB1, acc1_2);
    SB();
    bf16x8 wf13 = ldtr(wa6 + 16384, wa6 + 18432);
    acc2_2 = MF16(wf9, xB2, acc2_2);
    SB();
    acc3_2 = MF16(wf9, xB3, acc3_2);
    SB();
    acch_2 = MF16(wf9, xB4, acch_2);
    SB();
    acc0_3 = MF16(wf10, xB0, acc0_3);
    SB();
    acc1_3 = MF16(wf10, xB1, acc1_3);
    SB();
    asm volatile("s_waitcnt lgkmcnt(0)\n\ts_barrier" ::: "memory"); SB();
    const int d_ = sl == 2 ? -102400 : 51200; sl = sl == 2 ? 0 : sl + 1; xa0 += d_; xh0 += d_;
    wa0 += d_; bf16x8 wf14 = ldtr(wa0 + 0, wa0 + 2048);
    acc2_3 = MF16(wf10, xB2, acc2_3);
    SB();
    xA0 = ldx(xa0 + 0); xA1 = ldx(xa0 + 2048);
    acc3_3 = MF16(wf10, xB3, acc3_3);
    SB();
    acch_3 = MF16(wf10, xB4, acch_3);
    SB();
    xa1 += d_; xh1 += d_;
    acc0_4 = MF16(wf11, xB0, acc0_4);
    SB();
    wa4 += d_;
    acc1_4 = MF16(wf11, xB1, acc1_4);
    SB();
    wa1 += d_; bf16x8 wf15 = ldtr(wa1 + 0, wa1 + 2048);
    acc2_4 = MF16(wf11, xB2, acc2_4);
    SB();
    xA2 = ldx(xa0 + 4096); xA3 = ldx(xa0 + 6144);
    acc3_4 = MF16(wf11, xB3, acc3_4);
    SB();
    wa5 += d_;
    acc0_5 = MF16(wf12, xB0, acc0_5);
    SB();
    wa6 += d_;
    acc1_5 = MF16(wf12, xB1, acc1_5);
    SB();
    wa2 += d_; bf16x8 wf16 = ldtr(wa2 + 0, wa2 + 2048);
    acc2_5 = MF16(wf12, xB2, acc2_5);
    SB();
    xA4 = ldx(xh0);
    acc3_5 = MF16(wf12, xB3, acc3_5);
    SB();
    acc0_6 = MF16(wf13, xB0, acc0_6);
    SB();
    acc1_6 = MF16(wf13, xB1, acc1_6);
    SB();
    wa3 += d_; bf16x8 wf17 = ldtr(wa3 + 0, wa3 + 2048);
    acc2_6 = MF16(wf13, xB2, acc2_6);
    SB();
    acc3_6 = MF16(wf13, xB3, acc3_6);
    SB();
    wc0 = wf14; wc1 = wf15; wc2 = wf16; wc3 = wf17;
  }
  TS(3);
  TAG_FROM_LDS
  LATE_LANE(l_)
  if ((l_ & 15) == 0) *(LAS f32x4*)(lb + CS_OFF + cb0 * 64 + ((l_ >> 4) & 3) * 16) = acch_0;
  if ((l_ & 15) == 0) *(LAS f32x4*)(lb + CS_OFF + cb1 * 64 + ((l_ >> 4) & 3) * 16) = acch_1;
  if ((l_ & 15) == 0) *(LAS f32x4*)(lb + CS_OFF + cb2 * 64 + ((l_ >> 4) & 3) * 16) = acch_2;
  if ((l_ & 15) == 0) *(LAS f32x4*)(lb + CS_OFF + cb3 * 64 + ((l_ >> 4) & 3) * 16) = acch_3;
  TS(2);
  { const unsigned sta = lb + ST_OFF + (l_ & 15) * 8, csa = lb + CS_OFF + ((l_ >> 4) & 3) * 16;
    const unsigned ia = lb + 81920 + (l_ & 15) * 496 + ((l_ >> 4) & 3) * 8;
    const f32x2 eh = ldf2(sta); const f32x2 e0 = ldf2(sta + (1 + 4 * th + 0) * 128); const f32x2 e1 = ldf2(sta + (1 + 4 * th + 1) * 128); const f32x2 e2 = ldf2(sta + (1 + 4 * th + 2) * 128); const f32x2 e3 = ldf2(sta + (1 + 4 * th + 3) * 128);
    const unsigned ib = ia + (1 + 4 * th) * 7936;
    asm volatile("s_waitcnt lgkmcnt(0)\n\ts_barrier" ::: "memory"); SB();
    const f32x4 cv0 = ldf4(csa + cb0 * 64); const f32x4 cv1 = ldf4(csa + cb1 * 64); const f32x4 cv2 = ldf4(csa + cb2 * 64); const f32x4 cv3 = ldf4(csa + cb3 * 64); const f32x4 cv4 = ldf4(csa + cb4 * 64); const f32x4 cv5 = ldf4(csa + cb5 * 64); const f32x4 cv6 = ldf4(csa + cb6 * 64);
    { const f32x4 c_ = cv0; const unsigned a_ = ia + cb0 * 32, b_ = ib + cb0 * 32;
      *(LAS v2u*)(b_ + 0) = ln_pack(acc0_0, c_, e0);
      *(LAS v2u*)(b_ + 7936) = ln_pack(acc1_0, c_, e1);
      *(LAS v2u*)(b_ + 15872) = ln_pack(acc2_0, c_, e2);
      *(LAS v2u*)(b_ + 23808) = ln_pack(acc3_0, c_, e3);
      *(LAS v2u*)(a_) = ln_pack(acch_0, c_, eh);
    }
    { const f32x4 c_ = cv1; const unsigned a_ = ia + cb1 * 32, b_ = ib + cb1 * 32;
      *(LAS v2u*)(b_ + 0) = ln_pack(acc0_1, c_, e0);
      *(LAS v2u*)(b_ + 7936) = ln_pack(acc1_1, c_, e1);
      *(LAS v2u*)(b_ + 15872) = ln_pack(acc2_1, c_, e2);
      *(LAS v2u*)(b_ + 23808) = ln_pack(acc3_1, c_, e3);
      *(LAS v2u*)(a_) = ln_pack(acch_1, c_, eh);
    }
    { const f32x4 c_ = cv2; const unsigned a_ = ia + cb2 * 32, b_ = ib + cb2 * 32;
      *(LAS v2u*)(b_ + 0) = ln_pack(acc0_2, c_, e0);
      *(LAS v2u*)(b_ + 7936) = ln_pack(acc1_2, c_, e1);
      *(LAS v2u*)(b_ + 15872) = ln_pack(acc2_2, c_, e2);
      *(LAS v2u*)(b_ + 23808) = ln_pack(acc3_2, c_, e3);
      *(LAS v2u*)(a_) = ln_pack(acch_2, c_, eh);
    }
    { const f32x4 c_ = cv3; const unsigned a_ = ia + cb3 * 32, b_ = ib + cb3 * 32;
      *(LAS v2u*)(b_ + 0) = ln_pack(acc0_3, c_, e0);
      *(LAS v2u*)(b_ + 7936) = ln_pack(acc1_3, c_, e1);
      *(LAS v2u*)(b_ + 15872) = ln_pack(acc2_3, c_, e2);
      *(LAS v2u*)(b_ + 23808) = ln_pack(acc3_3, c_, e3);
      *(LAS v2u*)(a_) = ln_pack(acch_3, c_, eh);
    }
    { const f32x4 c_ = cv4; const unsigned a_ = ia + cb4 * 32, b_ = ib + cb4 * 32;
      *(LAS v2u*)(b_ + 0) = ln_pack(acc0_4, c_, e0);
      *(LAS v2u*)(b_ + 7936) = ln_pack(acc1_4, c_, e1);
      *(LAS v2u*)(b_ + 15872) = ln_pack(acc2_4, c_, e2);
      *(LAS v2u*)(b_ + 23808) = ln_pack(acc3_4, c_, e3);
    }
    { const f32x4 c_ = cv5; const unsigned a_ = ia + cb5 * 32, b_ = ib + cb5 * 32;
      *(LAS v2u*)(b_ + 0) = ln_pack(acc0_5, c_, e0);
      *(LAS v2u*)(b_ + 7936) = ln_pack(acc1_5, c_, e1);
      *(LAS v2u*)(b_ + 15872) = ln_pack(acc2_5, c_, e2);
      *(LAS v2u*)(b_ + 23808) = ln_pack(acc3_5, c_, e3);
    }
    { const f32x4 c_ = cv6; const unsigned a_ = ia + cb6 * 32, b_ = ib + cb6 * 32;
      *(LAS v2u*)(b_ + 0) = ln_pack(acc0_6, c_, e0);
      *(LAS v2u*)(b_ + 7936) = ln_pack(acc1_6, c_, e1);
      *(LAS v2u*)(b_ + 15872) = ln_pack(acc2_6, c_, e2);
      *(LAS v2u*)(b_ + 23808) = ln_pack(acc3_6, c_, e3);
    }
  }
  TS(4);
  } else if (rp == 1) {
  const int rbase = pn * 128;
  const unsigned xoff32 = (lane >> 3) * 1280 + (((lane & 7) ^ (lane >> 4) ^ ((cg & 1) << 2)) << 4);
  const unsigned woff = (lane >> 3) * 3072 + ((((lane >> 2) & 1) ^ ((lane >> 4) & 1)) << 6) + (lane & 3) * 16;
  const char* wsrc = (const char*)w1 + ct * 384 + (size_t)(8 * cg) * 3072;
  const unsigned wd = lb + cg * 3072;
  const char* xs0 = (const char*)x + (size_t)(rbase + 8 * cg) * 1280;
  unsigned so = 0; int kc = ROT0;
#define LOAD_A { const char* sw_ = wsrc + (size_t)kc * 3072; DMA(xoff32, xs0 + kc * 2 + 0, lb + so + 32768 + cg * 1024); DMA(xoff32, xs0 + kc * 2 + 40960, lb + so + 36864 + cg * 1024); DMA(xoff32, xs0 + kc * 2 + 81920, lb + so + 40960 + cg * 1024); DMA(xoff32, xs0 + kc * 2 + 122880, lb + so + 45056 + cg * 1024); DMA(woff, sw_ + 0, wd + so + 0); DMA(woff, sw_ + 128, wd + so + 1024); }
#define LOAD_B { const char* sw_ = wsrc + (size_t)kc * 3072; DMA(woff, sw_ + 256, wd + so + 2048); DMA(woff, sw_ + 98304, wd + so + 12288); DMA(woff, sw_ + 98432, wd + so + 13312); DMA(woff, sw_ + 98560, wd + so + 14336); kc = KNEXT; so = so == 102400 ? 0 : so + 51200; }
  TS(14);
  LOAD_A LOAD_B
  TS(2);
  LOAD_A
  WBAR(6);
  TS(1);
  LOAD_B
  ROW_LOAD2
  LOAD_A
  WBAR(9);
  WARM2
  STATS_PRODUCE2(7);
#pragma unroll 1
  for (int it = 2; it < 9; ++it) {
    LOAD_B LOAD_A
    WBAR(6);
  }
  LOAD_B
#undef LOAD_A
#undef LOAD_B
  const unsigned stoff = min(max(rbase + (int)(threadIdx.x & 255), 0), 2047) * 16;
  v4u stv; asm volatile("global_load_dwordx4 %0, %1, %2 sc1" : "=v"(stv) : "v"(stoff), "s"(statp) : "memory");

  const int p3valid = (tk >> 10) & 1, ct3 = (tk >> 11) & 15;
  const unsigned w3off = (lane >> 3) * 1280 + ((((lane >> 1) & 3) ^ ((lane >> 4) & 1)) << 5) + (lane & 1) * 16;
  const unsigned w3off1 = w3off ^ 64;
  const char* w3h = (const char*)w2 + (size_t)(cg * 384) * 1280 + ct3 * 128;
  const char* w3o = (const char*)wo + (size_t)(cg * 160) * 1280 + ct3 * 128;

  W3_LOAD(0, 65536)
  WBAR(5);
  asm volatile("s_waitcnt vmcnt(4)" : "+v"(stv) :: "memory"); STATS_CONSUME(1);
  asm volatile("s_waitcnt lgkmcnt(0)\n\ts_barrier" ::: "memory");
  asm volatile("s_barrier" ::: "memory");
  { const int th = cg & 1, ch = cg >> 1; LATE_LANE(l_)
    const unsigned sta = lb + ST_OFF + (th * 64 + (l_ & 31)) * 8, csa = lb + CS_OFF + ((l_ >> 5) & 1) * 16;
    const v4u orsrc = make_rsrc(ws + WS_ACT_OFF, 512u * AKS * 128u);
    const unsigned ovoff = ((l_ & 31) >> 2) * (AKS * 128) + (l_ & 3) * 32 + ((l_ >> 5) & 1) * 16;
    const unsigned osoff = (pn * 128 + th * 64) * (AKS * 32) + ct * 12 * 128;

    f32x16 hacc0; { const unsigned a_ = lb + 98304 + cg * 4096 + (l_ & 63) * 16; _Pragma("unroll") for (int q = 0; q < 4; ++q) { const f32x4 t_ = ldf4(a_ + q * 1024); hacc0[4 * q] = t_[0]; hacc0[4 * q + 1] = t_[1]; hacc0[4 * q + 2] = t_[2]; hacc0[4 * q + 3] = t_[3]; } }
    f32x16 hacc1; { const unsigned a_ = lb + 114688 + cg * 4096 + (l_ & 63) * 16; _Pragma("unroll") for (int q = 0; q < 4; ++q) { const f32x4 t_ = ldf4(a_ + q * 1024); hacc1[4 * q] = t_[0]; hacc1[4 * q + 1] = t_[1]; hacc1[4 * q + 2] = t_[2]; hacc1[4 * q + 3] = t_[3]; } }
    f32x16 hacc2; { const unsigned a_ = lb + 131072 + cg * 4096 + (l_ & 63) * 16; _Pragma("unroll") for (int q = 0; q < 4; ++q) { const f32x4 t_ = ldf4(a_ + q * 1024); hacc2[4 * q] = t_[0]; hacc2[4 * q + 1] = t_[1]; hacc2[4 * q + 2] = t_[2]; hacc2[4 * q + 3] = t_[3]; } }
    asm volatile("s_waitcnt lgkmcnt(0)" : "+v"(hacc0), "+v"(hacc1), "+v"(hacc2) :: "memory");
    TS(2);
    const f32x2 e1 = ldf2(sta + 256);
    { f32x4 cv[4], bv[4]; _Pragma("unroll") for (int q = 0; q < 4; ++q) { const unsigned a_ = csa + (ch * 3 + 0) * 128 + q * 32; cv[q] = ldf4(a_) + ldf4(a_ + 768); bv[q] = ldf4(a_ + 1536); }
      gelu_store32(hacc0, cv, bv, e1, orsrc, ovoff, osoff + 32 * (AKS * 32) + (ch * 3 + 0) * 256); }
    { f32x4 cv[4], bv[4]; _Pragma("unroll") for (int q = 0; q < 4; ++q) { const unsigned a_ = csa + (ch * 3 + 1) * 128 + q * 32; cv[q] = ldf4(a_) + ldf4(a_ + 768); bv[q] = ldf4(a_ + 1536); }
      gelu_store32(hacc1, cv, bv, e1, orsrc, ovoff, osoff + 32 * (AKS * 32) + (ch * 3 + 1) * 256); }
    { f32x4 cv[4], bv[4]; _Pragma("unroll") for (int q = 0; q < 4; ++q) { const unsigned a_ = csa + (ch * 3 + 2) * 128 + q * 32; cv[q] = ldf4(a_) + ldf4(a_ + 768); bv[q] = ldf4(a_ + 1536); }
      gelu_store32(hacc2, cv, bv, e1, orsrc, ovoff, osoff + 32 * (AKS * 32) + (ch * 3 + 2) * 256); }
  }
  TS(3);
  asm volatile("s_waitcnt vmcnt(0)" ::: "memory");
  if (lane == 0) { const unsigned n_ = __hip_atomic_fetch_add((LAS unsigned*)(lb + TAG_OFF + 8), 1u, __ATOMIC_RELAXED, __HIP_MEMORY_SCOPE_WORKGROUP);
    if (n_ == 7) __hip_atomic_store((GAS u64*)(ws + WS_FLAG1_OFF) + 128 + pn * 8 + ct, tag, __ATOMIC_RELAXED, __HIP_MEMORY_SCOPE_AGENT); }
  TS(8);
  W3_LOAD(6, 98304) W3_LOAD(7, 114688) W3_LOAD(8, 131072)
  TS(11);
  } else {
  const int th = cg & 1, ch = cg >> 1, t32 = lane & 31, hh = lane >> 5;
  { const int i_ = ch * 96 + th * 48 + (lane < 48 ? lane : 0), r_ = i_ & 31, G_ = r_ >> 2, q_ = G_ >> 1, h_ = G_ & 1;
    const unsigned o_ = (ct * 192 + (i_ & ~31) + 4 * (4 * (q_ >> 1) + 2 * h_ + (q_ & 1)) + (r_ & 3)) * 2;
    asm volatile("s_mov_b32 m0, %2\n\ts_nop 0\n\tglobal_load_lds_ushort %0, %1" :: "v"(o_), "s"(b1), "s"(lb + B1_OFF + cg * 256) : "memory"); }
  WARM
  unsigned xa0 = (lb + 32768 + th * 8192 + t32 * 128 + (((hh ^ (t32 >> 1)) & 7) << 4)) ^ (th << 5);
  unsigned xa1 = xa0 ^ 32, xa2 = xa0 ^ 64, xa3 = xa0 ^ 96;
  const int q_ = (lane >> 2) & 3, p_ = lane & 3;
  const unsigned wb_ = lb + (g >> 1) * 3072 + q_ * 128 + (g & 1) * 32 + (2 * (p_ & 1) + (p_ >> 1)) * 8;
  unsigned we0 = wb_ + ((ch * 3 + 0) >> 1) * 1024 + ((((ch * 3 + 0) & 1) ^ (q_ >> 1)) << 6) + th * 6144, wo0 = we0 + 6144 - th * 12288;
  unsigned we1 = wb_ + ((ch * 3 + 1) >> 1) * 1024 + ((((ch * 3 + 1) & 1) ^ (q_ >> 1)) << 6) + th * 6144, wo1 = we1 + 6144 - th * 12288;
  unsigned we2 = wb_ + ((ch * 3 + 2) >> 1) * 1024 + ((((ch * 3 + 2) & 1) ^ (q_ >> 1)) << 6) + th * 6144, wo2 = we2 + 6144 - th * 12288;
  const unsigned sel_ = (t16 == (g & 1)) ? 0x3f803f80u : 0u; const v4u selv = {sel_, sel_, sel_, sel_};
  f32x4 cs0 = {0.f, 0.f, 0.f, 0.f}, cs1 = cs0, cs2 = cs0;
  f32x16 acc0_0 = {}; f32x16 acc0_1 = {}; f32x16 acc0_2 = {}; f32x16 acc1_0 = {}; f32x16 acc1_1 = {}; f32x16 acc1_2 = {};
  bf16x8 xA0, xA1, wA0, wA1, wA2, xB0, xB1, wB0, wB1, wB2, xC0, xC1, wC0, wC1, wC2, xD0, xD1, wD0, wD1, wD2;
  TS(9);
  asm volatile("s_barrier" ::: "memory"); SB();
  TS(1);
  wA0 = ldtr(we0 + 0, we0 + 512); SB();
  xA0 = ldx(xa0); xA1 = ldx(xa0 + 4096); SB();
  wA1 = ldtr(we1 + 0, we1 + 512); SB();
  wA2 = ldtr(we2 + 0, we2 + 512); SB();
  wB0 = ldtr(wo0 + 0, wo0 + 512); SB();
  xB0 = ldx(xa1); xB1 = ldx(xa1 + 4096); SB();
  wB1 = ldtr(wo1 + 0, wo1 + 512); SB();
  wB2 = ldtr(wo2 + 0, wo2 + 512); SB();
  int sl = 0;
#pragma unroll 1
  for (int j = 0; j < 10; ++j) {
    acc0_0 = MF32(wA0, xA0, acc0_0);
    SB();
    wC0 = ldtr(we0 + 12288, we0 + 12800);
    acc1_0 = MF32(wA0, xA1, acc1_0);
    cs0 = MF16(wA0, __builtin_bit_cast(bf16x8, selv), cs0);
    SB();
    xC0 = ldx(xa2); xC1 = ldx(xa2 + 4096);
    acc0_1 = MF32(wA1, xA0, acc0_1);
    SB();
    wC1 = ldtr(we1 + 12288, we1 + 12800);
    acc1_1 = MF32(wA1, xA1, acc1_1);
    cs1 = MF16(wA1, __builtin_bit_cast(bf16x8, selv), cs1);
    SB();
    wC2 = ldtr(we2 + 12288, we2 + 12800);
    acc0_2 = MF32(wA2, xA0, acc0_2);
    SB();
    acc1_2 = MF32(wA2, xA1, acc1_2);
    cs2 = MF16(wA2, __builtin_bit_cast(bf16x8, selv), cs2);
    SB();
    acc0_0 = MF32(wB0, xB0, acc0_0);
    SB();
    wD0 = ldtr(wo0 + 12288, wo0 + 12800);
    acc1_0 = MF32(wB0, xB1, acc1_0);
    SB();
    xD0 = ldx(xa3); xD1 = ldx(xa3 + 4096);
    acc0_1 = MF32(wB1, xB0, acc0_1);
    SB();
    wD1 = ldtr(wo1 + 12288, wo1 + 12800);
    acc1_1 = MF32(wB1, xB1, acc1_1);
    SB();
    wD2 = ldtr(wo2 + 12288, wo2 + 12800);
    acc0_2 = MF32(wB2, xB0, acc0_2);
    SB();
    acc1_2 = MF32(wB2, xB1, acc1_2);
    SB();
    acc0_0 = MF32(wC0, xC0, acc0_0);
    SB();
    asm volatile("s_waitcnt lgkmcnt(0)\n\ts_barrier" ::: "memory"); SB();
    const int d_ = sl == 2 ? -102400 : 51200; sl = sl == 2 ? 0 : sl + 1; xa0 += d_; we0 += d_; we1 += d_; we2 += d_;
    wA0 = ldtr(we0 + 0, we0 + 512);
    acc1_0 = MF32(wC0, xC1, acc1_0);
    cs0 = MF16(wC0, __builtin_bit_cast(bf16x8, selv), cs0);
    SB();
    xA0 = ldx(xa0); xA1 = ldx(xa0 + 4096);
    acc0_1 = MF32(wC1, xC0, acc0_1);
    SB();
    wA1 = ldtr(we1 + 0, we1 + 512);
    acc1_1 = MF32(wC1, xC1, acc1_1);
    cs1 = MF16(wC1, __builtin_bit_cast(bf16x8, selv), cs1);
    xa1 += d_; xa2 += d_; xa3 += d_;
    SB();
    wA2 = ldtr(we2 + 0, we2 + 512);
    acc0_2 = MF32(wC2, xC0, acc0_2);
    wo0 += d_; wo1 += d_; wo2 += d_;
    SB();
    acc1_2 = MF32(wC2, xC1, acc1_2);
    cs2 = MF16(wC2, __builtin_bit_cast(bf16x8, selv), cs2);
    SB();
    acc0_0 = MF32(wD0, xD0, acc0_0);
    SB();
    wB0 = ldtr(wo0 + 0, wo0 + 512);
    acc1_0 = MF32(wD0, xD1, acc1_0);
    SB();
    xB0 = ldx(xa1); xB1 = ldx(xa1 + 4096);
    acc0_1 = MF32(wD1, xD0, acc0_1);
    SB();
    wB1 = ldtr(wo1 + 0, wo1 + 512);
    acc1_1 = MF32(wD1, xD1, acc1_1);
    SB();
    wB2 = ldtr(wo2 + 0, wo2 + 512);
    acc0_2 = MF32(wD2, xD0, acc0_2);
    SB();
    acc1_2 = MF32(wD2, xD1, acc1_2);
    SB();
  }
  TS(3);
  TAG_FROM_LDS
  LATE_LANE(l_)
  if ((l_ & 15) < 2) { const unsigned a_ = lb + CS_OFF + th * 768 + ch * 384 + (l_ & 1) * 64 + ((l_ >> 4) & 3) * 16; *(LAS f32x4*)(a_) = cs0; *(LAS f32x4*)(a_ + 128) = cs1; *(LAS f32x4*)(a_ + 256) = cs2; }
  asm volatile("s_waitcnt vmcnt(0)" ::: "memory");
  { const unsigned b1raw = *(LAS u16*)(lb + B1_OFF + cg * 256 + (l_ & 63) * 4); if ((l_ & 63) < 48) *(LAS float*)(lb + CS_OFF + 1536 + (ch * 96 + th * 48 + (l_ & 63)) * 4) = __uint_as_float(b1raw << 16); }
  { const unsigned a_ = lb + 98304 + cg * 4096 + (l_ & 63) * 16; *(LAS f32x4*)(a_ + 0) = (f32x4){acc1_0[0], acc1_0[1], acc1_0[2], acc1_0[3]}; *(LAS f32x4*)(a_ + 1024) = (f32x4){acc1_0[4], acc1_0[5], acc1_0[6], acc1_0[7]}; *(LAS f32x4*)(a_ + 2048) = (f32x4){acc1_0[8], acc1_0[9], acc1_0[10], acc1_0[11]}; *(LAS f32x4*)(a_ + 3072) = (f32x4){acc1_0[12], acc1_0[13], acc1_0[14], acc1_0[15]}; }
  { const unsigned a_ = lb + 114688 + cg * 4096 + (l_ & 63) * 16; *(LAS f32x4*)(a_ + 0) = (f32x4){acc1_1[0], acc1_1[1], acc1_1[2], acc1_1[3]}; *(LAS f32x4*)(a_ + 1024) = (f32x4){acc1_1[4], acc1_1[5], acc1_1[6], acc1_1[7]}; *(LAS f32x4*)(a_ + 2048) = (f32x4){acc1_1[8], acc1_1[9], acc1_1[10], acc1_1[11]}; *(LAS f32x4*)(a_ + 3072) = (f32x4){acc1_1[12], acc1_1[13], acc1_1[14], acc1_1[15]}; }
  { const unsigned a_ = lb + 131072 + cg * 4096 + (l_ & 63) * 16; *(LAS f32x4*)(a_ + 0) = (f32x4){acc1_2[0], acc1_2[1], acc1_2[2], acc1_2[3]}; *(LAS f32x4*)(a_ + 1024) = (f32x4){acc1_2[4], acc1_2[5], acc1_2[6], acc1_2[7]}; *(LAS f32x4*)(a_ + 2048) = (f32x4){acc1_2[8], acc1_2[9], acc1_2[10], acc1_2[11]}; *(LAS f32x4*)(a_ + 3072) = (f32x4){acc1_2[12], acc1_2[13], acc1_2[14], acc1_2[15]}; }
  asm volatile("s_waitcnt lgkmcnt(0)\n\ts_barrier" ::: "memory"); SB();
  {
    const unsigned sta = lb + ST_OFF + (th * 64 + (l_ & 31)) * 8, csa = lb + CS_OFF + ((l_ >> 5) & 1) * 16;
    const v4u orsrc = make_rsrc(ws + WS_ACT_OFF, 512u * AKS * 128u);
    const unsigned ovoff = ((l_ & 31) >> 2) * (AKS * 128) + (l_ & 3) * 32 + ((l_ >> 5) & 1) * 16;
    const unsigned osoff = (pn * 128 + th * 64) * (AKS * 32) + ct * 12 * 128;

    const f32x2 e0 = ldf2(sta + 0);
    { f32x4 cv[4], bv[4]; _Pragma("unroll") for (int q = 0; q < 4; ++q) { const unsigned a_ = csa + (ch * 3 + 0) * 128 + q * 32; cv[q] = ldf4(a_) + ldf4(a_ + 768); bv[q] = ldf4(a_ + 1536); }
      gelu_store32(acc0_0, cv, bv, e0, orsrc, ovoff, osoff + 0 * (AKS * 32) + (ch * 3 + 0) * 256); }
    { f32x4 cv[4], bv[4]; _Pragma("unroll") for (int q = 0; q < 4; ++q) { const unsigned a_ = csa + (ch * 3 + 1) * 128 + q * 32; cv[q] = ldf4(a_) + ldf4(a_ + 768); bv[q] = ldf4(a_ + 1536); }
      gelu_store32(acc0_1, cv, bv, e0, orsrc, ovoff, osoff + 0 * (AKS * 32) + (ch * 3 + 1) * 256); }
    { f32x4 cv[4], bv[4]; _Pragma("unroll") for (int q = 0; q < 4; ++q) { const unsigned a_ = csa + (ch * 3 + 2) * 128 + q * 32; cv[q] = ldf4(a_) + ldf4(a_ + 768); bv[q] = ldf4(a_ + 1536); }
      gelu_store32(acc0_2, cv, bv, e0, orsrc, ovoff, osoff + 0 * (AKS * 32) + (ch * 3 + 2) * 256); }
  }
  TS(4);
  asm volatile("s_waitcnt vmcnt(0)" ::: "memory");
  if (lane == 0) { const unsigned n_ = __hip_atomic_fetch_add((LAS unsigned*)(lb + TAG_OFF + 8), 1u, __ATOMIC_RELAXED, __HIP_MEMORY_SCOPE_WORKGROUP);
    if (n_ == 7) __hip_atomic_store((GAS u64*)(ws + WS_FLAG1_OFF) + 128 + pn * 8 + ct, tag, __ATOMIC_RELAXED, __HIP_MEMORY_SCOPE_AGENT); }
  TS(8);
  {
  const int p3valid = (tk >> 10) & 1, ct3 = (tk >> 11) & 15;
  const unsigned w3off = (lane >> 3) * 1280 + ((((lane >> 1) & 3) ^ ((lane >> 4) & 1)) << 5) + (lane & 1) * 16;
  const unsigned w3off1 = w3off ^ 64;
  const char* w3h = (const char*)w2 + (size_t)(cg * 384) * 1280 + ct3 * 128;
  const char* w3o = (const char*)wo + (size_t)(cg * 160) * 1280 + ct3 * 128;
  W3_LOAD(1, 0) W3_LOAD(2, 16384) W3_LOAD(3, 32768) W3_LOAD(4, 49152) W3_LOAD(5, 81920) }
  TS(11);
  }

  const int p3valid = (tk >> 10) & 1, ct3 = (tk >> 11) & 15;
  const unsigned w3off = (lane >> 3) * 1280 + ((((lane >> 1) & 3) ^ ((lane >> 4) & 1)) << 5) + (lane & 1) * 16;
  const unsigned w3off1 = w3off ^ 64;
  const char* w3h = (const char*)w2 + (size_t)(cg * 384) * 1280 + ct3 * 128;
  const char* w3o = (const char*)wo + (size_t)(cg * 160) * 1280 + ct3 * 128;

  if (typ == 0) {
    asm volatile("s_waitcnt lgkmcnt(0)\n\ts_barrier" ::: "memory");
    TS(8);
    ATTENTION
    TS(11);
    ATTENTION2
  }
  const int r0 = ((tk >> 15) & 511) * 4, nr3 = ((tk >> 24) & 31) * 4;
  const int kq = cg, mh = rp;
  GAS u64* flagO = (GAS u64*)(ws + WS_FLAG1_OFF);
  GAS u64* flagH = flagO + 128;
  const v4u ars = make_rsrc(ws + WS_ACT_OFF, p3valid ? 512u * AKS * 128u : 0u);
  const int tl0 = mh ? (nr3 > 64 ? 48 : 32) : 0;
  const int ntb = (mh ? nr3 - tl0 : nr3) > (mh ? 32 : 64) ? 3 : 2;
  const unsigned pa = lb + (kq * 96 + tl0 + t16) * 272 + g * 16;
  v4u xv0, xv1, bv;
  unsigned av[3];
#pragma unroll
  for (int tb = 0; tb < 3; ++tb) { int tl = tl0 + tb * 16 + t16; tl = tl < nr3 ? tl : nr3 - 1; const int tok = r0 + tl;
    av[tb] = ((tok >> 2) * AKS + (g >> 1)) * 128 + (tok & 3) * 32 + (g & 1) * 16; }
  const unsigned soh = kq * 12 * 256, soo = (48 + kq * 5) * 256;
  unsigned wa3;
  { const int q = (lane >> 2) & 3, p = lane & 3; wa3 = lb + kq * 4096 + (8 * g + q) * 128 + (((q >> 1) | ((g & 1) << 1)) << 5) + p * 8; }
  f32x4 acc[3][4];
#pragma unroll
  for (int tb = 0; tb < 3; ++tb)
#pragma unroll
    for (int cb = 0; cb < 4; ++cb) acc[tb][cb] = (f32x4){0.f, 0.f, 0.f, 0.f};
  { const int l_ = lane & 15; const int pn_ = (l_ & 8) ? (r0 + nr3 - 1) >> 7 : r0 >> 7; GAS u64* fp = flagH + pn_ * 8 + (l_ & 7); POLL(fp, w - 4); }
  TS(14);
  asm volatile("s_waitcnt vmcnt(0)\n\ts_barrier" ::: "memory");
  if (typ == 0 && threadIdx.x == 0) __hip_atomic_store(flagO + pn * 8 + ct, tag, __ATOMIC_RELAXED, __HIP_MEMORY_SCOPE_AGENT);
  TS(5);
  TS(10);
#define FINAL_PREFETCH v4u xv0_, xv1_, bv_; { LATE_LANE(lf_) const int tla = min(lf_ >> 3, nr3 - 1), tl1 = min(64 + (lf_ >> 3), nr3 - 1); \
    const unsigned o0_ = (r0 + tla) * 1280 + ct3 * 128 + (lf_ & 7) * 16, o1_ = (r0 + tl1) * 1280 + ct3 * 128 + (lf_ & 7) * 16, ob_ = ct3 * 128 + (lf_ & 7) * 16; \
    asm volatile("global_load_dwordx4 %0, %3, %6\n\tglobal_load_dwordx4 %1, %4, %6\n\tglobal_load_dwordx4 %2, %5, %7" : "=&v"(xv0_), "=&v"(xv1_), "=&v"(bv_) : "v"(o0_), "v"(o1_), "v"(ob_), "s"(x), "s"(b2) : "memory"); }

  if (typ == 0) { W3_LOADH(5, 81920) W3_LOADH(6, 98304) W3_LOADH(7, 114688) W3_LOADH(8, 131072) }
  if (ntb == 3) {
  bf16x8 a0_0, a0_1, a0_2; { const unsigned so_ = soh + 0;
    asm volatile("buffer_load_dwordx4 %0, %3, %6, %7 offen sc1\n\tbuffer_load_dwordx4 %1, %4, %6, %7 offen sc1\n\tbuffer_load_dwordx4 %2, %5, %6, %7 offen sc1" : "=&v"(a0_0), "=&v"(a0_1), "=&v"(a0_2) : "v"(av[0]), "v"(av[1]), "v"(av[2]), "s"(ars), "s"(so_) : "memory"); }
  bf16x8 a1_0, a1_1, a1_2; { const unsigned so_ = soh + 256;
    asm volatile("buffer_load_dwordx4 %0, %3, %6, %7 offen sc1\n\tbuffer_load_dwordx4 %1, %4, %6, %7 offen sc1\n\tbuffer_load_dwordx4 %2, %5, %6, %7 offen sc1" : "=&v"(a1_0), "=&v"(a1_1), "=&v"(a1_2) : "v"(av[0]), "v"(av[1]), "v"(av[2]), "s"(ars), "s"(so_) : "memory"); }
  bf16x8 a2_0, a2_1, a2_2; { const unsigned so_ = soh + 512;
    asm volatile("buffer_load_dwordx4 %0, %3, %6, %7 offen sc1\n\tbuffer_load_dwordx4 %1, %4, %6, %7 offen sc1\n\tbuffer_load_dwordx4 %2, %5, %6, %7 offen sc1" : "=&v"(a2_0), "=&v"(a2_1), "=&v"(a2_2) : "v"(av[0]), "v"(av[1]), "v"(av[2]), "s"(ars), "s"(so_) : "memory"); }
  bf16x8 a3_0, a3_1, a3_2; { const unsigned so_ = soh + 768;
    asm volatile("buffer_load_dwordx4 %0, %3, %6, %7 offen sc1\n\tbuffer_load_dwordx4 %1, %4, %6, %7 offen sc1\n\tbuffer_load_dwordx4 %2, %5, %6, %7 offen sc1" : "=&v"(a3_0), "=&v"(a3_1), "=&v"(a3_2) : "v"(av[0]), "v"(av[1]), "v"(av[2]), "s"(ars), "s"(so_) : "memory"); }
  OPAQUE(wa3); const unsigned wb0 = wa3 + 65536u;
  bf16x8 w0_0 = ldtr((wb0 ^ 0u), (wb0 ^ 0u) + 512);
  bf16x8 w0_1 = ldtr((wb0 ^ 32u), (wb0 ^ 32u) + 512);
  bf16x8 w0_2 = ldtr((wb0 ^ 64u), (wb0 ^ 64u) + 512);
  bf16x8 w0_3 = ldtr((wb0 ^ 96u), (wb0 ^ 96u) + 512);
  bf16x8 a4_0, a4_1, a4_2; { const unsigned so_ = soh + 1024;
    asm volatile("buffer_load_dwordx4 %0, %3, %6, %7 offen sc1\n\tbuffer_load_dwordx4 %1, %4, %6, %7 offen sc1\n\tbuffer_load_dwordx4 %2, %5, %6, %7 offen sc1" : "=&v"(a4_0), "=&v"(a4_1), "=&v"(a4_2) : "v"(av[0]), "v"(av[1]), "v"(av[2]), "s"(ars), "s"(so_) : "memory"); }
  asm volatile("s_waitcnt vmcnt(12)" : "+v"(a0_0), "+v"(a0_1), "+v"(a0_2) :: "memory"); SB();
  acc[0][0] = MF16(w0_0, a0_0, acc[0][0]);
  acc[1][0] = MF16(w0_0, a0_1, acc[1][0]);
  acc[2][0] = MF16(w0_0, a0_2, acc[2][0]);
  acc[0][1] = MF16(w0_1, a0_0, acc[0][1]);
  acc[1][1] = MF16(w0_1, a0_1, acc[1][1]);
  acc[2][1] = MF16(w0_1, a0_2, acc[2][1]);
  SB();
  OPAQUE(wa3); const unsigned wb1 = wa3 + 0u;
  bf16x8 w1_0 = ldtr((wb1 ^ 0u), (wb1 ^ 0u) + 512);
  bf16x8 w1_1 = ldtr((wb1 ^ 32u), (wb1 ^ 32u) + 512);
  bf16x8 w1_2 = ldtr((wb1 ^ 64u), (wb1 ^ 64u) + 512);
  bf16x8 w1_3 = ldtr((wb1 ^ 96u), (wb1 ^ 96u) + 512);
  SB();
  acc[0][2] = MF16(w0_2, a0_0, acc[0][2]);
  acc[1][2] = MF16(w0_2, a0_1, acc[1][2]);
  acc[2][2] = MF16(w0_2, a0_2, acc[2][2]);
  acc[0][3] = MF16(w0_3, a0_0, acc[0][3]);
  acc[1][3] = MF16(w0_3, a0_1, acc[1][3]);
  acc[2][3] = MF16(w0_3, a0_2, acc[2][3]);
  SB();
  bf16x8 a5_0, a5_1, a5_2; { const unsigned so_ = soh + 1280;
    asm volatile("buffer_load_dwordx4 %0, %3, %6, %7 offen sc1\n\tbuffer_load_dwordx4 %1, %4, %6, %7 offen sc1\n\tbuffer_load_dwordx4 %2, %5, %6, %7 offen sc1" : "=&v"(a5_0), "=&v"(a5_1), "=&v"(a5_2) : "v"(av[0]), "v"(av[1]), "v"(av[2]), "s"(ars), "s"(so_) : "memory"); }
  asm volatile("s_waitcnt vmcnt(12)" : "+v"(a1_0), "+v"(a1_1), "+v"(a1_2) :: "memory"); SB();
  acc[0][0] = MF16(w1_0, a1_0, acc[0][0]);
  acc[1][0] = MF16(w1_0, a1_1, acc[1][0]);
  acc[2][0] = MF16(w1_0, a1_2, acc[2][0]);
  acc[0][1] = MF16(w1_1, a1_0, acc[0][1]);
  acc[1][1] = MF16(w1_1, a1_1, acc[1][1]);
  acc[2][1] = MF16(w1_1, a1_2, acc[2][1]);
  SB();
  OPAQUE(wa3); const unsigned wb2 = wa3 + 16384u;
  bf16x8 w2_0 = ldtr((wb2 ^ 0u), (wb2 ^ 0u) + 512);
  bf16x8 w2_1 = ldtr((wb2 ^ 32u), (wb2 ^ 32u) + 512);
  bf16x8 w2_2 = ldtr((wb2 ^ 64u), (wb2 ^ 64u) + 512);
  bf16x8 w2_3 = ldtr((wb2 ^ 96u), (wb2 ^ 96u) + 512);
  SB();
  acc[0][2] = MF16(w1_2, a1_0, acc[0][2]);
  acc[1][2] = MF16(w1_2, a1_1, acc[1][2]);
  acc[2][2] = MF16(w1_2, a1_2, acc[2][2]);
  acc[0][3] = MF16(w1_3, a1_0, acc[0][3]);
  acc[1][3] = MF16(w1_3, a1_1, acc[1][3]);
  acc[2][3] = MF16(w1_3, a1_2, acc[2][3]);
  SB();
  bf16x8 a6_0, a6_1, a6_2; { const unsigned so_ = soh + 1536;
    asm volatile("buffer_load_dwordx4 %0, %3, %6, %7 offen sc1\n\tbuffer_load_dwordx4 %1, %4, %6, %7 offen sc1\n\tbuffer_load_dwordx4 %2, %5, %6, %7 offen sc1" : "=&v"(a6_0), "=&v"(a6_1), "=&v"(a6_2) : "v"(av[0]), "v"(av[1]), "v"(av[2]), "s"(ars), "s"(so_) : "memory"); }
  asm volatile("s_waitcnt vmcnt(12)" : "+v"(a2_0), "+v"(a2_1), "+v"(a2_2) :: "memory"); SB();
  acc[0][0] = MF16(w2_0, a2_0, acc[0][0]);
  acc[1][0] = MF16(w2_0, a2_1, acc[1][0]);
  acc[2][0] = MF16(w2_0, a2_2, acc[2][0]);
  acc[0][1] = MF16(w2_1, a2_0, acc[0][1]);
  acc[1][1] = MF16(w2_1, a2_1, acc[1][1]);
  acc[2][1] = MF16(w2_1, a2_2, acc[2][1]);
  SB();
  OPAQUE(wa3); const unsigned wb3 = wa3 + 32768u;
  bf16x8 w3_0 = ldtr((wb3 ^ 0u), (wb3 ^ 0u) + 512);
  bf16x8 w3_1 = ldtr((wb3 ^ 32u), (wb3 ^ 32u) + 512);
  bf16x8 w3_2 = ldtr((wb3 ^ 64u), (wb3 ^ 64u) + 512);
  bf16x8 w3_3 = ldtr((wb3 ^ 96u), (wb3 ^ 96u) + 512);
  SB();
  acc[0][2] = MF16(w2_2, a2_0, acc[0][2]);
  acc[1][2] = MF16(w2_2, a2_1, acc[1][2]);
  acc[2][2] = MF16(w2_2, a2_2, acc[2][2]);
  acc[0][3] = MF16(w2_3, a2_0, acc[0][3]);
  acc[1][3] = MF16(w2_3, a2_1, acc[1][3]);
  acc[2][3] = MF16(w2_3, a2_2, acc[2][3]);
  SB();
  bf16x8 a7_0, a7_1, a7_2; { const unsigned so_ = soh + 1792;
    asm volatile("buffer_load_dwordx4 %0, %3, %6, %7 offen sc1\n\tbuffer_load_dwordx4 %1, %4, %6, %7 offen sc1\n\tbuffer_load_dwordx4 %2, %5, %6, %7 offen sc1" : "=&v"(a7_0), "=&v"(a7_1), "=&v"(a7_2) : "v"(av[0]), "v"(av[1]), "v"(av[2]), "s"(ars), "s"(so_) : "memory"); }
  asm volatile("s_waitcnt vmcnt(12)" : "+v"(a3_0), "+v"(a3_1), "+v"(a3_2) :: "memory"); SB();
  acc[0][0] = MF16(w3_0, a3_0, acc[0][0]);
  acc[1][0] = MF16(w3_0, a3_1, acc[1][0]);
  acc[2][0] = MF16(w3_0, a3_2, acc[2][0]);
  acc[0][1] = MF16(w3_1, a3_0, acc[0][1]);
  acc[1][1] = MF16(w3_1, a3_1, acc[1][1]);
  acc[2][1] = MF16(w3_1, a3_2, acc[2][1]);
  SB();
  asm volatile("s_waitcnt vmcnt(63) lgkmcnt(0)\n\ts_barrier" ::: "memory"); SB();
  W3_LOADH(9, 65536)
  W3_LOADH(10, 0)
  W3_LOADH(11, 16384)
  W3_LOADH(12, 32768)
  OPAQUE(wa3); const unsigned wb4 = wa3 + 49152u;
  bf16x8 w4_0 = ldtr((wb4 ^ 0u), (wb4 ^ 0u) + 512);
  bf16x8 w4_1 = ldtr((wb4 ^ 32u), (wb4 ^ 32u) + 512);
  bf16x8 w4_2 = ldtr((wb4 ^ 64u), (wb4 ^ 64u) + 512);
  bf16x8 w4_3 = ldtr((wb4 ^ 96u), (wb4 ^ 96u) + 512);
  SB();
  acc[0][2] = MF16(w3_2, a3_0, acc[0][2]);
  acc[1][2] = MF16(w3_2, a3_1, acc[1][2]);
  acc[2][2] = MF16(w3_2, a3_2, acc[2][2]);
  acc[0][3] = MF16(w3_3, a3_0, acc[0][3]);
  acc[1][3] = MF16(w3_3, a3_1, acc[1][3]);
  acc[2][3] = MF16(w3_3, a3_2, acc[2][3]);
  SB();
  bf16x8 a8_0, a8_1, a8_2; { const unsigned so_ = soh + 2048;
    asm volatile("buffer_load_dwordx4 %0, %3, %6, %7 offen sc1\n\tbuffer_load_dwordx4 %1, %4, %6, %7 offen sc1\n\tbuffer_load_dwordx4 %2, %5, %6, %7 offen sc1" : "=&v"(a8_0), "=&v"(a8_1), "=&v"(a8_2) : "v"(av[0]), "v"(av[1]), "v"(av[2]), "s"(ars), "s"(so_) : "memory"); }
  asm volatile("s_waitcnt vmcnt(20)" : "+v"(a4_0), "+v"(a4_1), "+v"(a4_2) :: "memory"); SB();
  acc[0][0] = MF16(w4_0, a4_0, acc[0][0]);
  acc[1][0] = MF16(w4_0, a4_1, acc[1][0]);
  acc[2][0] = MF16(w4_0, a4_2, acc[2][0]);
  acc[0][1] = MF16(w4_1, a4_0, acc[0][1]);
  acc[1][1] = MF16(w4_1, a4_1, acc[1][1]);
  acc[2][1] = MF16(w4_1, a4_2, acc[2][1]);
  SB();
  OPAQUE(wa3); const unsigned wb5 = wa3 + 81920u;
  bf16x8 w5_0 = ldtr((wb5 ^ 0u), (wb5 ^ 0u) + 512);
  bf16x8 w5_1 = ldtr((wb5 ^ 32u), (wb5 ^ 32u) + 512);
  bf16x8 w5_2 = ldtr((wb5 ^ 64u), (wb5 ^ 64u) + 512);
  bf16x8 w5_3 = ldtr((wb5 ^ 96u), (wb5 ^ 96u) + 512);
  SB();
  acc[0][2] = MF16(w4_2, a4_0, acc[0][2]);
  acc[1][2] = MF16(w4_2, a4_1, acc[1][2]);
  acc[2][2] = MF16(w4_2, a4_2, acc[2][2]);
  acc[0][3] = MF16(w4_3, a4_0, acc[0][3]);
  acc[1][3] = MF16(w4_3, a4_1, acc[1][3]);
  acc[2][3] = MF16(w4_3, a4_2, acc[2][3]);
  SB();
  bf16x8 a9_0, a9_1, a9_2; { const unsigned so_ = soh + 2304;
    asm volatile("buffer_load_dwordx4 %0, %3, %6, %7 offen sc1\n\tbuffer_load_dwordx4 %1, %4, %6, %7 offen sc1\n\tbuffer_load_dwordx4 %2, %5, %6, %7 offen sc1" : "=&v"(a9_0), "=&v"(a9_1), "=&v"(a9_2) : "v"(av[0]), "v"(av[1]), "v"(av[2]), "s"(ars), "s"(so_) : "memory"); }
  asm volatile("s_waitcnt vmcnt(20)" : "+v"(a5_0), "+v"(a5_1), "+v"(a5_2) :: "memory"); SB();
  acc[0][0] = MF16(w5_0, a5_0, acc[0][0]);
  acc[1][0] = MF16(w5_0, a5_1, acc[1][0]);
  acc[2][0] = MF16(w5_0, a5_2, acc[2][0]);
  acc[0][1] = MF16(w5_1, a5_0, acc[0][1]);
  acc[1][1] = MF16(w5_1, a5_1, acc[1][1]);
  acc[2][1] = MF16(w5_1, a5_2, acc[2][1]);
  SB();
  OPAQUE(wa3); const unsigned wb6 = wa3 + 98304u;
  bf16x8 w6_0 = ldtr((wb6 ^ 0u), (wb6 ^ 0u) + 512);
  bf16x8 w6_1 = ldtr((wb6 ^ 32u), (wb6 ^ 32u) + 512);
  bf16x8 w6_2 = ldtr((wb6 ^ 64u), (wb6 ^ 64u) + 512);
  bf16x8 w6_3 = ldtr((wb6 ^ 96u), (wb6 ^ 96u) + 512);
  SB();
  acc[0][2] = MF16(w5_2, a5_0, acc[0][2]);
  acc[1][2] = MF16(w5_2, a5_1, acc[1][2]);
  acc[2][2] = MF16(w5_2, a5_2, acc[2][2]);
  acc[0][3] = MF16(w5_3, a5_0, acc[0][3]);
  acc[1][3] = MF16(w5_3, a5_1, acc[1][3]);
  acc[2][3] = MF16(w5_3, a5_2, acc[2][3]);
  SB();
  bf16x8 a10_0, a10_1, a10_2; { const unsigned so_ = soh + 2560;
    asm volatile("buffer_load_dwordx4 %0, %3, %6, %7 offen sc1\n\tbuffer_load_dwordx4 %1, %4, %6, %7 offen sc1\n\tbuffer_load_dwordx4 %2, %5, %6, %7 offen sc1" : "=&v"(a10_0), "=&v"(a10_1), "=&v"(a10_2) : "v"(av[0]), "v"(av[1]), "v"(av[2]), "s"(ars), "s"(so_) : "memory"); }
  asm volatile("s_waitcnt vmcnt(20)" : "+v"(a6_0), "+v"(a6_1), "+v"(a6_2) :: "memory"); SB();
  acc[0][0] = MF16(w6_0, a6_0, acc[0][0]);
  acc[1][0] = MF16(w6_0, a6_1, acc[1][0]);
  acc[2][0] = MF16(w6_0, a6_2, acc[2][0]);
  acc[0][1] = MF16(w6_1, a6_0, acc[0][1]);
  acc[1][1] = MF16(w6_1, a6_1, acc[1][1]);
  acc[2][1] = MF16(w6_1, a6_2, acc[2][1]);
  SB();
  OPAQUE(wa3); const unsigned wb7 = wa3 + 114688u;
  bf16x8 w7_0 = ldtr((wb7 ^ 0u), (wb7 ^ 0u) + 512);
  bf16x8 w7_1 = ldtr((wb7 ^ 32u), (wb7 ^ 32u) + 512);
  bf16x8 w7_2 = ldtr((wb7 ^ 64u), (wb7 ^ 64u) + 512);
  bf16x8 w7_3 = ldtr((wb7 ^ 96u), (wb7 ^ 96u) + 512);
  SB();
  acc[0][2] = MF16(w6_2, a6_0, acc[0][2]);
  acc[1][2] = MF16(w6_2, a6_1, acc[1][2]);
  acc[2][2] = MF16(w6_2, a6_2, acc[2][2]);
  acc[0][3] = MF16(w6_3, a6_0, acc[0][3]);
  acc[1][3] = MF16(w6_3, a6_1, acc[1][3]);
  acc[2][3] = MF16(w6_3, a6_2, acc[2][3]);
  SB();
  bf16x8 a11_0, a11_1, a11_2; { const unsigned so_ = soh + 2816;
    asm volatile("buffer_load_dwordx4 %0, %3, %6, %7 offen sc1\n\tbuffer_load_dwordx4 %1, %4, %6, %7 offen sc1\n\tbuffer_load_dwordx4 %2, %5, %6, %7 offen sc1" : "=&v"(a11_0), "=&v"(a11_1), "=&v"(a11_2) : "v"(av[0]), "v"(av[1]), "v"(av[2]), "s"(ars), "s"(so_) : "memory"); }
  asm volatile("s_waitcnt vmcnt(20)" : "+v"(a7_0), "+v"(a7_1), "+v"(a7_2) :: "memory"); SB();
  acc[0][0] = MF16(w7_0, a7_0, acc[0][0]);
  acc[1][0] = MF16(w7_0, a7_1, acc[1][0]);
  acc[2][0] = MF16(w7_0, a7_2, acc[2][0]);
  acc[0][1] = MF16(w7_1, a7_0, acc[0][1]);
  acc[1][1] = MF16(w7_1, a7_1, acc[1][1]);
  acc[2][1] = MF16(w7_1, a7_2, acc[2][1]);
  SB();
  { const int l_ = lane & 15; const int pn_ = (l_ & 8) ? (r0 + nr3 - 1) >> 7 : r0 >> 7; GAS u64* fp = flagO + pn_ * 8 + (l_ & 7); POLL(fp, w); }
  TS(12);
  asm volatile("s_waitcnt vmcnt(12) lgkmcnt(0)\n\ts_barrier" ::: "memory"); SB();
  W3_LOADH(13, 49152)
  W3_LOADH(14, 81920)
  W3_LOADH(15, 98304)
  W3_LOADH(16, 114688)
  OPAQUE(wa3); const unsigned wb8 = wa3 + 131072u;
  bf16x8 w8_0 = ldtr((wb8 ^ 0u), (wb8 ^ 0u) + 512);
  bf16x8 w8_1 = ldtr((wb8 ^ 32u), (wb8 ^ 32u) + 512);
  bf16x8 w8_2 = ldtr((wb8 ^ 64u), (wb8 ^ 64u) + 512);
  bf16x8 w8_3 = ldtr((wb8 ^ 96u), (wb8 ^ 96u) + 512);
  SB();
  acc[0][2] = MF16(w7_2, a7_0, acc[0][2]);
  acc[1][2] = MF16(w7_2, a7_1, acc[1][2]);
  acc[2][2] = MF16(w7_2, a7_2, acc[2][2]);
  acc[0][3] = MF16(w7_3, a7_0, acc[0][3]);
  acc[1][3] = MF16(w7_3, a7_1, acc[1][3]);
  acc[2][3] = MF16(w7_3, a7_2, acc[2][3]);
  SB();
  bf16x8 a12_0, a12_1, a12_2; { const unsigned so_ = soo + 0;
    asm volatile("buffer_load_dwordx4 %0, %3, %6, %7 offen sc1\n\tbuffer_load_dwordx4 %1, %4, %6, %7 offen sc1\n\tbuffer_load_dwordx4 %2, %5, %6, %7 offen sc1" : "=&v"(a12_0), "=&v"(a12_1), "=&v"(a12_2) : "v"(av[0]), "v"(av[1]), "v"(av[2]), "s"(ars), "s"(so_) : "memory"); }
  asm volatile("s_waitcnt vmcnt(20)" : "+v"(a8_0), "+v"(a8_1), "+v"(a8_2) :: "memory"); SB();
  acc[0][0] = MF16(w8_0, a8_0, acc[0][0]);
  acc[1][0] = MF16(w8_0, a8_1, acc[1][0]);
  acc[2][0] = MF16(w8_0, a8_2, acc[2][0]);
  acc[0][1] = MF16(w8_1, a8_0, acc[0][1]);
  acc[1][1] = MF16(w8_1, a8_1, acc[1][1]);
  acc[2][1] = MF16(w8_1, a8_2, acc[2][1]);
  SB();
  OPAQUE(wa3); const unsigned wb9 = wa3 + 65536u;
  bf16x8 w9_0 = ldtr((wb9 ^ 0u), (wb9 ^ 0u) + 512);
  bf16x8 w9_1 = ldtr((wb9 ^ 32u), (wb9 ^ 32u) + 512);
  bf16x8 w9_2 = ldtr((wb9 ^ 64u), (wb9 ^ 64u) + 512);
  bf16x8 w9_3 = ldtr((wb9 ^ 96u), (wb9 ^ 96u) + 512);
  SB();
  acc[0][2] = MF16(w8_2, a8_0, acc[0][2]);
  acc[1][2] = MF16(w8_2, a8_1, acc[1][2]);
  acc[2][2] = MF16(w8_2, a8_2, acc[2][2]);
  acc[0][3] = MF16(w8_3, a8_0, acc[0][3]);
  acc[1][3] = MF16(w8_3, a8_1, acc[1][3]);
  acc[2][3] = MF16(w8_3, a8_2, acc[2][3]);
  SB();
  bf16x8 a13_0, a13_1, a13_2; { const unsigned so_ = soo + 256;
    asm volatile("buffer_load_dwordx4 %0, %3, %6, %7 offen sc1\n\tbuffer_load_dwordx4 %1, %4, %6, %7 offen sc1\n\tbuffer_load_dwordx4 %2, %5, %6, %7 offen sc1" : "=&v"(a13_0), "=&v"(a13_1), "=&v"(a13_2) : "v"(av[0]), "v"(av[1]), "v"(av[2]), "s"(ars), "s"(so_) : "memory"); }
  asm volatile("s_waitcnt vmcnt(20)" : "+v"(a9_0), "+v"(a9_1), "+v"(a9_2) :: "memory"); SB();
  acc[0][0] = MF16(w9_0, a9_0, acc[0][0]);
  acc[1][0] = MF16(w9_0, a9_1, acc[1][0]);
  acc[2][0] = MF16(w9_0, a9_2, acc[2][0]);
  acc[0][1] = MF16(w9_1, a9_0, acc[0][1]);
  acc[1][1] = MF16(w9_1, a9_1, acc[1][1]);
  acc[2][1] = MF16(w9_1, a9_2, acc[2][1]);
  SB();
  OPAQUE(wa3); const unsigned wb10 = wa3 + 0u;
  bf16x8 w10_0 = ldtr((wb10 ^ 0u), (wb10 ^ 0u) + 512);
  bf16x8 w10_1 = ldtr((wb10 ^ 32u), (wb10 ^ 32u) + 512);
  bf16x8 w10_2 = ldtr((wb10 ^ 64u), (wb10 ^ 64u) + 512);
  bf16x8 w10_3 = ldtr((wb10 ^ 96u), (wb10 ^ 96u) + 512);
  SB();
  acc[0][2] = MF16(w9_2, a9_0, acc[0][2]);
  acc[1][2] = MF16(w9_2, a9_1, acc[1][2]);
  acc[2][2] = MF16(w9_2, a9_2, acc[2][2]);
  acc[0][3] = MF16(w9_3, a9_0, acc[0][3]);
  acc[1][3] = MF16(w9_3, a9_1, acc[1][3]);
  acc[2][3] = MF16(w9_3, a9_2, acc[2][3]);
  SB();
  bf16x8 a14_0, a14_1, a14_2; { const unsigned so_ = soo + 512;
    asm volatile("buffer_load_dwordx4 %0, %3, %6, %7 offen sc1\n\tbuffer_load_dwordx4 %1, %4, %6, %7 offen sc1\n\tbuffer_load_dwordx4 %2, %5, %6, %7 offen sc1" : "=&v"(a14_0), "=&v"(a14_1), "=&v"(a14_2) : "v"(av[0]), "v"(av[1]), "v"(av[2]), "s"(ars), "s"(so_) : "memory"); }
  asm volatile("s_waitcnt vmcnt(20)" : "+v"(a10_0), "+v"(a10_1), "+v"(a10_2) :: "memory"); SB();
  acc[0][0] = MF16(w10_0, a10_0, acc[0][0]);
  acc[1][0] = MF16(w10_0, a10_1, acc[1][0]);
  acc[2][0] = MF16(w10_0, a10_2, acc[2][0]);
  acc[0][1] = MF16(w10_1, a10_0, acc[0][1]);
  acc[1][1] = MF16(w10_1, a10_1, acc[1][1]);
  acc[2][1] = MF16(w10_1, a10_2, acc[2][1]);
  SB();
  OPAQUE(wa3); const unsigned wb11 = wa3 + 16384u;
  bf16x8 w11_0 = ldtr((wb11 ^ 0u), (wb11 ^ 0u) + 512);
  bf16x8 w11_1 = ldtr((wb11 ^ 32u), (wb11 ^ 32u) + 512);
  bf16x8 w11_2 = ldtr((wb11 ^ 64u), (wb11 ^ 64u) + 512);
  bf16x8 w11_3 = ldtr((wb11 ^ 96u), (wb11 ^ 96u) + 512);
  SB();
  acc[0][2] = MF16(w10_2, a10_0, acc[0][2]);
  acc[1][2] = MF16(w10_2, a10_1, acc[1][2]);
  acc[2][2] = MF16(w10_2, a10_2, acc[2][2]);
  acc[0][3] = MF16(w10_3, a10_0, acc[0][3]);
  acc[1][3] = MF16(w10_3, a10_1, acc[1][3]);
  acc[2][3] = MF16(w10_3, a10_2, acc[2][3]);
  SB();
  bf16x8 a15_0, a15_1, a15_2; { const unsigned so_ = soo + 768;
    asm volatile("buffer_load_dwordx4 %0, %3, %6, %7 offen sc1\n\tbuffer_load_dwordx4 %1, %4, %6, %7 offen sc1\n\tbuffer_load_dwordx4 %2, %5, %6, %7 offen sc1" : "=&v"(a15_0), "=&v"(a15_1), "=&v"(a15_2) : "v"(av[0]), "v"(av[1]), "v"(av[2]), "s"(ars), "s"(so_) : "memory"); }
  asm volatile("s_waitcnt vmcnt(20)" : "+v"(a11_0), "+v"(a11_1), "+v"(a11_2) :: "memory"); SB();
  acc[0][0] = MF16(w11_0, a11_0, acc[0][0]);
  acc[1][0] = MF16(w11_0, a11_1, acc[1][0]);
  acc[2][0] = MF16(w11_0, a11_2, acc[2][0]);
  acc[0][1] = MF16(w11_1, a11_0, acc[0][1]);
  acc[1][1] = MF16(w11_1, a11_1, acc[1][1]);
  acc[2][1] = MF16(w11_1, a11_2, acc[2][1]);
  SB();
  OPAQUE(wa3); const unsigned wb12 = wa3 + 32768u;
  bf16x8 w12_0 = ldtr((wb12 ^ 0u), (wb12 ^ 0u) + 512);
  bf16x8 w12_1 = ldtr((wb12 ^ 32u), (wb12 ^ 32u) + 512);
  bf16x8 w12_2 = ldtr((wb12 ^ 64u), (wb12 ^ 64u) + 512);
  bf16x8 w12_3 = ldtr((wb12 ^ 96u), (wb12 ^ 96u) + 512);
  SB();
  acc[0][2] = MF16(w11_2, a11_0, acc[0][2]);
  acc[1][2] = MF16(w11_2, a11_1, acc[1][2]);
  acc[2][2] = MF16(w11_2, a11_2, acc[2][2]);
  acc[0][3] = MF16(w11_3, a11_0, acc[0][3]);
  acc[1][3] = MF16(w11_3, a11_1, acc[1][3]);
  acc[2][3] = MF16(w11_3, a11_2, acc[2][3]);
  SB();
  bf16x8 a16_0, a16_1, a16_2; { const unsigned so_ = soo + 1024;
    asm volatile("buffer_load_dwordx4 %0, %3, %6, %7 offen sc1\n\tbuffer_load_dwordx4 %1, %4, %6, %7 offen sc1\n\tbuffer_load_dwordx4 %2, %5, %6, %7 offen sc1" : "=&v"(a16_0), "=&v"(a16_1), "=&v"(a16_2) : "v"(av[0]), "v"(av[1]), "v"(av[2]), "s"(ars), "s"(so_) : "memory"); }
  asm volatile("s_waitcnt vmcnt(12)" : "+v"(a12_0), "+v"(a12_1), "+v"(a12_2) :: "memory"); SB();
  acc[0][0] = MF16(w12_0, a12_0, acc[0][0]);
  acc[1][0] = MF16(w12_0, a12_1, acc[1][0]);
  acc[2][0] = MF16(w12_0, a12_2, acc[2][0]);
  acc[0][1] = MF16(w12_1, a12_0, acc[0][1]);
  acc[1][1] = MF16(w12_1, a12_1, acc[1][1]);
  acc[2][1] = MF16(w12_1, a12_2, acc[2][1]);
  SB();
  asm volatile("s_waitcnt vmcnt(15) lgkmcnt(0)\n\ts_barrier" ::: "memory"); SB();
  OPAQUE(wa3); const unsigned wb13 = wa3 + 49152u;
  bf16x8 w13_0 = ldtr((wb13 ^ 0u), (wb13 ^ 0u) + 512);
  bf16x8 w13_1 = ldtr((wb13 ^ 32u), (wb13 ^ 32u) + 512);
  bf16x8 w13_2 = ldtr((wb13 ^ 64u), (wb13 ^ 64u) + 512);
  bf16x8 w13_3 = ldtr((wb13 ^ 96u), (wb13 ^ 96u) + 512);
  SB();
  acc[0][2] = MF16(w12_2, a12_0, acc[0][2]);
  acc[1][2] = MF16(w12_2, a12_1, acc[1][2]);
  acc[2][2] = MF16(w12_2, a12_2, acc[2][2]);
  acc[0][3] = MF16(w12_3, a12_0, acc[0][3]);
  acc[1][3] = MF16(w12_3, a12_1, acc[1][3]);
  acc[2][3] = MF16(w12_3, a12_2, acc[2][3]);
  SB();
  FINAL_PREFETCH
  asm volatile("s_waitcnt vmcnt(12)" : "+v"(a13_0), "+v"(a13_1), "+v"(a13_2) :: "memory"); SB();
  acc[0][0] = MF16(w13_0, a13_0, acc[0][0]);
  acc[1][0] = MF16(w13_0, a13_1, acc[1][0]);
  acc[2][0] = MF16(w13_0, a13_2, acc[2][0]);
  acc[0][1] = MF16(w13_1, a13_0, acc[0][1]);
  acc[1][1] = MF16(w13_1, a13_1, acc[1][1]);
  acc[2][1] = MF16(w13_1, a13_2, acc[2][1]);
  SB();
  OPAQUE(wa3); const unsigned wb14 = wa3 + 81920u;
  bf16x8 w14_0 = ldtr((wb14 ^ 0u), (wb14 ^ 0u) + 512);
  bf16x8 w14_1 = ldtr((wb14 ^ 32u), (wb14 ^ 32u) + 512);
  bf16x8 w14_2 = ldtr((wb14 ^ 64u), (wb14 ^ 64u) + 512);
  bf16x8 w14_3 = ldtr((wb14 ^ 96u), (wb14 ^ 96u) + 512);
  SB();
  acc[0][2] = MF16(w13_2, a13_0, acc[0][2]);
  acc[1][2] = MF16(w13_2, a13_1, acc[1][2]);
  acc[2][2] = MF16(w13_2, a13_2, acc[2][2]);
  acc[0][3] = MF16(w13_3, a13_0, acc[0][3]);
  acc[1][3] = MF16(w13_3, a13_1, acc[1][3]);
  acc[2][3] = MF16(w13_3, a13_2, acc[2][3]);
  SB();
  asm volatile("s_waitcnt vmcnt(9)" : "+v"(a14_0), "+v"(a14_1), "+v"(a14_2) :: "memory"); SB();
  acc[0][0] = MF16(w14_0, a14_0, acc[0][0]);
  acc[1][0] = MF16(w14_0, a14_1, acc[1][0]);
  acc[2][0] = MF16(w14_0, a14_2, acc[2][0]);
  acc[0][1] = MF16(w14_1, a14_0, acc[0][1]);
  acc[1][1] = MF16(w14_1, a14_1, acc[1][1]);
  acc[2][1] = MF16(w14_1, a14_2, acc[2][1]);
  SB();
  OPAQUE(wa3); const unsigned wb15 = wa3 + 98304u;
  bf16x8 w15_0 = ldtr((wb15 ^ 0u), (wb15 ^ 0u) + 512);
  bf16x8 w15_1 = ldtr((wb15 ^ 32u), (wb15 ^ 32u) + 512);
  bf16x8 w15_2 = ldtr((wb15 ^ 64u), (wb15 ^ 64u) + 512);
  bf16x8 w15_3 = ldtr((wb15 ^ 96u), (wb15 ^ 96u) + 512);
  SB();
  acc[0][2] = MF16(w14_2, a14_0, acc[0][2]);
  acc[1][2] = MF16(w14_2, a14_1, acc[1][2]);
  acc[2][2] = MF16(w14_2, a14_2, acc[2][2]);
  acc[0][3] = MF16(w14_3, a14_0, acc[0][3]);
  acc[1][3] = MF16(w14_3, a14_1, acc[1][3]);
  acc[2][3] = MF16(w14_3, a14_2, acc[2][3]);
  SB();
  asm volatile("s_waitcnt vmcnt(6)" : "+v"(a15_0), "+v"(a15_1), "+v"(a15_2) :: "memory"); SB();
  acc[0][0] = MF16(w15_0, a15_0, acc[0][0]);
  acc[1][0] = MF16(w15_0, a15_1, acc[1][0]);
  acc[2][0] = MF16(w15_0, a15_2, acc[2][0]);
  acc[0][1] = MF16(w15_1, a15_0, acc[0][1]);
  acc[1][1] = MF16(w15_1, a15_1, acc[1][1]);
  acc[2][1] = MF16(w15_1, a15_2, acc[2][1]);
  SB();
  OPAQUE(wa3); const unsigned wb16 = wa3 + 114688u;
  bf16x8 w16_0 = ldtr((wb16 ^ 0u), (wb16 ^ 0u) + 512);
  bf16x8 w16_1 = ldtr((wb16 ^ 32u), (wb16 ^ 32u) + 512);
  bf16x8 w16_2 = ldtr((wb16 ^ 64u), (wb16 ^ 64u) + 512);
  bf16x8 w16_3 = ldtr((wb16 ^ 96u), (wb16 ^ 96u) + 512);
  SB();
  acc[0][2] = MF16(w15_2, a15_0, acc[0][2]);
  acc[1][2] = MF16(w15_2, a15_1, acc[1][2]);
  acc[2][2] = MF16(w15_2, a15_2, acc[2][2]);
  acc[0][3] = MF16(w15_3, a15_0, acc[0][3]);
  acc[1][3] = MF16(w15_3, a15_1, acc[1][3]);
  acc[2][3] = MF16(w15_3, a15_2, acc[2][3]);
  SB();
  asm volatile("s_waitcnt vmcnt(3)" : "+v"(a16_0), "+v"(a16_1), "+v"(a16_2) :: "memory"); SB();
  acc[0][0] = MF16(w16_0, a16_0, acc[0][0]);
  acc[1][0] = MF16(w16_0, a16_1, acc[1][0]);
  acc[2][0] = MF16(w16_0, a16_2, acc[2][0]);
  acc[0][1] = MF16(w16_1, a16_0, acc[0][1]);
  acc[1][1] = MF16(w16_1, a16_1, acc[1][1]);
  acc[2][1] = MF16(w16_1, a16_2, acc[2][1]);
  SB();
  SB();
  acc[0][2] = MF16(w16_2, a16_0, acc[0][2]);
  acc[1][2] = MF16(w16_2, a16_1, acc[1][2]);
  acc[2][2] = MF16(w16_2, a16_2, acc[2][2]);
  acc[0][3] = MF16(w16_3, a16_0, acc[0][3]);
  acc[1][3] = MF16(w16_3, a16_1, acc[1][3]);
  acc[2][3] = MF16(w16_3, a16_2, acc[2][3]);
  SB();
  TS(13);
  asm volatile("s_waitcnt vmcnt(0)\n\ts_barrier" : "+v"(xv0_), "+v"(xv1_), "+v"(bv_) :: "memory"); xv0 = xv0_; xv1 = xv1_; bv = bv_;
  *(LAS f32x4*)(pa + 0) = acc[0][0];
  *(LAS f32x4*)(pa + 64) = acc[0][1];
  *(LAS f32x4*)(pa + 128) = acc[0][2];
  *(LAS f32x4*)(pa + 192) = acc[0][3];
  *(LAS f32x4*)(pa + 4352) = acc[1][0];
  *(LAS f32x4*)(pa + 4416) = acc[1][1];
  *(LAS f32x4*)(pa + 4480) = acc[1][2];
  *(LAS f32x4*)(pa + 4544) = acc[1][3];
  *(LAS f32x4*)(pa + 8704) = acc[2][0];
  *(LAS f32x4*)(pa + 8768) = acc[2][1];
  *(LAS f32x4*)(pa + 8832) = acc[2][2];
  *(LAS f32x4*)(pa + 8896) = acc[2][3];
  } else {
  bf16x8 a0_0, a0_1; { const unsigned so_ = soh + 0;
    asm volatile("buffer_load_dwordx4 %0, %2, %4, %5 offen sc1\n\tbuffer_load_dwordx4 %1, %3, %4, %5 offen sc1" : "=&v"(a0_0), "=&v"(a0_1) : "v"(av[0]), "v"(av[1]), "s"(ars), "s"(so_) : "memory"); }
  bf16x8 a1_0, a1_1; { const unsigned so_ = soh + 256;
    asm volatile("buffer_load_dwordx4 %0, %2, %4, %5 offen sc1\n\tbuffer_load_dwordx4 %1, %3, %4, %5 offen sc1" : "=&v"(a1_0), "=&v"(a1_1) : "v"(av[0]), "v"(av[1]), "s"(ars), "s"(so_) : "memory"); }
  bf16x8 a2_0, a2_1; { const unsigned so_ = soh + 512;
    asm volatile("buffer_load_dwordx4 %0, %2, %4, %5 offen sc1\n\tbuffer_load_dwordx4 %1, %3, %4, %5 offen sc1" : "=&v"(a2_0), "=&v"(a2_1) : "v"(av[0]), "v"(av[1]), "s"(ars), "s"(so_) : "memory"); }
  bf16x8 a3_0, a3_1; { const unsigned so_ = soh + 768;
    asm volatile("buffer_load_dwordx4 %0, %2, %4, %5 offen sc1\n\tbuffer_load_dwordx4 %1, %3, %4, %5 offen sc1" : "=&v"(a3_0), "=&v"(a3_1) : "v"(av[0]), "v"(av[1]), "s"(ars), "s"(so_) : "memory"); }
  OPAQUE(wa3); const unsigned wb0 = wa3 + 65536u;
  bf16x8 w0_0 = ldtr((wb0 ^ 0u), (wb0 ^ 0u) + 512);
  bf16x8 w0_1 = ldtr((wb0 ^ 32u), (wb0 ^ 32u) + 512);
  bf16x8 w0_2 = ldtr((wb0 ^ 64u), (wb0 ^ 64u) + 512);
  bf16x8 w0_3 = ldtr((wb0 ^ 96u), (wb0 ^ 96u) + 512);
  bf16x8 a4_0, a4_1; { const unsigned so_ = soh + 1024;
    asm volatile("buffer_load_dwordx4 %0, %2, %4, %5 offen sc1\n\tbuffer_load_dwordx4 %1, %3, %4, %5 offen sc1" : "=&v"(a4_0), "=&v"(a4_1) : "v"(av[0]), "v"(av[1]), "s"(ars), "s"(so_) : "memory"); }
  asm volatile("s_waitcnt vmcnt(8)" : "+v"(a0_0), "+v"(a0_1) :: "memory"); SB();
  acc[0][0] = MF16(w0_0, a0_0, acc[0][0]);
  acc[1][0] = MF16(w0_0, a0_1, acc[1][0]);
  acc[0][1] = MF16(w0_1, a0_0, acc[0][1]);
  acc[1][1] = MF16(w0_1, a0_1, acc[1][1]);
  SB();
  OPAQUE(wa3); const unsigned wb1 = wa3 + 0u;
  bf16x8 w1_0 = ldtr((wb1 ^ 0u), (wb1 ^ 0u) + 512);
  bf16x8 w1_1 = ldtr((wb1 ^ 32u), (wb1 ^ 32u) + 512);
  bf16x8 w1_2 = ldtr((wb1 ^ 64u), (wb1 ^ 64u) + 512);
  bf16x8 w1_3 = ldtr((wb1 ^ 96u), (wb1 ^ 96u) + 512);
  SB();
  acc[0][2] = MF16(w0_2, a0_0, acc[0][2]);
  acc[1][2] = MF16(w0_2, a0_1, acc[1][2]);
  acc[0][3] = MF16(w0_3, a0_0, acc[0][3]);
  acc[1][3] = MF16(w0_3, a0_1, acc[1][3]);
  SB();
  bf16x8 a5_0, a5_1; { const unsigned so_ = soh + 1280;
    asm volatile("buffer_load_dwordx4 %0, %2, %4, %5 offen sc1\n\tbuffer_load_dwordx4 %1, %3, %4, %5 offen sc1" : "=&v"(a5_0), "=&v"(a5_1) : "v"(av[0]), "v"(av[1]), "s"(ars), "s"(so_) : "memory"); }
  asm volatile("s_waitcnt vmcnt(8)" : "+v"(a1_0), "+v"(a1_1) :: "memory"); SB();
  acc[0][0] = MF16(w1_0, a1_0, acc[0][0]);
  acc[1][0] = MF16(w1_0, a1_1, acc[1][0]);
  acc[0][1] = MF16(w1_1, a1_0, acc[0][1]);
  acc[1][1] = MF16(w1_1, a1_1, acc[1][1]);
  SB();
  OPAQUE(wa3); const unsigned wb2 = wa3 + 16384u;
  bf16x8 w2_0 = ldtr((wb2 ^ 0u), (wb2 ^ 0u) + 512);
  bf16x8 w2_1 = ldtr((wb2 ^ 32u), (wb2 ^ 32u) + 512);
  bf16x8 w2_2 = ldtr((wb2 ^ 64u), (wb2 ^ 64u) + 512);
  bf16x8 w2_3 = ldtr((wb2 ^ 96u), (wb2 ^ 96u) + 512);
  SB();
  acc[0][2] = MF16(w1_2, a1_0, acc[0][2]);
  acc[1][2] = MF16(w1_2, a1_1, acc[1][2]);
  acc[0][3] = MF16(w1_3, a1_0, acc[0][3]);
  acc[1][3] = MF16(w1_3, a1_1, acc[1][3]);
  SB();
  bf16x8 a6_0, a6_1; { const unsigned so_ = soh + 1536;
    asm volatile("buffer_load_dwordx4 %0, %2, %4, %5 offen sc1\n\tbuffer_load_dwordx4 %1, %3, %4, %5 offen sc1" : "=&v"(a6_0), "=&v"(a6_1) : "v"(av[0]), "v"(av[1]), "s"(ars), "s"(so_) : "memory"); }
  asm volatile("s_waitcnt vmcnt(8)" : "+v"(a2_0), "+v"(a2_1) :: "memory"); SB();
  acc[0][0] = MF16(w2_0, a2_0, acc[0][0]);
  acc[1][0] = MF16(w2_0, a2_1, acc[1][0]);
  acc[0][1] = MF16(w2_1, a2_0, acc[0][1]);
  acc[1][1] = MF16(w2_1, a2_1, acc[1][1]);
  SB();
  OPAQUE(wa3); const unsigned wb3 = wa3 + 32768u;
  bf16x8 w3_0 = ldtr((wb3 ^ 0u), (wb3 ^ 0u) + 512);
  bf16x8 w3_1 = ldtr((wb3 ^ 32u), (wb3 ^ 32u) + 512);
  bf16x8 w3_2 = ldtr((wb3 ^ 64u), (wb3 ^ 64u) + 512);
  bf16x8 w3_3 = ldtr((wb3 ^ 96u), (wb3 ^ 96u) + 512);
  SB();
  acc[0][2] = MF16(w2_2, a2_0, acc[0][2]);
  acc[1][2] = MF16(w2_2, a2_1, acc[1][2]);
  acc[0][3] = MF16(w2_3, a2_0, acc[0][3]);
  acc[1][3] = MF16(w2_3, a2_1, acc[1][3]);
  SB();
  bf16x8 a7_0, a7_1; { const unsigned so_ = soh + 1792;
    asm volatile("buffer_load_dwordx4 %0, %2, %4, %5 offen sc1\n\tbuffer_load_dwordx4 %1, %3, %4, %5 offen sc1" : "=&v"(a7_0), "=&v"(a7_1) : "v"(av[0]), "v"(av[1]), "s"(ars), "s"(so_) : "memory"); }
  asm volatile("s_waitcnt vmcnt(8)" : "+v"(a3_0), "+v"(a3_1) :: "memory"); SB();
  acc[0][0] = MF16(w3_0, a3_0, acc[0][0]);
  acc[1][0] = MF16(w3_0, a3_1, acc[1][0]);
  acc[0][1] = MF16(w3_1, a3_0, acc[0][1]);
  acc[1][1] = MF16(w3_1, a3_1, acc[1][1]);
  SB();
  asm volatile("s_waitcnt vmcnt(63) lgkmcnt(0)\n\ts_barrier" ::: "memory"); SB();
  W3_LOADH(9, 65536)
  W3_LOADH(10, 0)
  W3_LOADH(11, 16384)
  W3_LOADH(12, 32768)
  OPAQUE(wa3); const unsigned wb4 = wa3 + 49152u;
  bf16x8 w4_0 = ldtr((wb4 ^ 0u), (wb4 ^ 0u) + 512);
  bf16x8 w4_1 = ldtr((wb4 ^ 32u), (wb4 ^ 32u) + 512);
  bf16x8 w4_2 = ldtr((wb4 ^ 64u), (wb4 ^ 64u) + 512);
  bf16x8 w4_3 = ldtr((wb4 ^ 96u), (wb4 ^ 96u) + 512);
  SB();
  acc[0][2] = MF16(w3_2, a3_0, acc[0][2]);
  acc[1][2] = MF16(w3_2, a3_1, acc[1][2]);
  acc[0][3] = MF16(w3_3, a3_0, acc[0][3]);
  acc[1][3] = MF16(w3_3, a3_1, acc[1][3]);
  SB();
  bf16x8 a8_0, a8_1; { const unsigned so_ = soh + 2048;
    asm volatile("buffer_load_dwordx4 %0, %2, %4, %5 offen sc1\n\tbuffer_load_dwordx4 %1, %3, %4, %5 offen sc1" : "=&v"(a8_0), "=&v"(a8_1) : "v"(av[0]), "v"(av[1]), "s"(ars), "s"(so_) : "memory"); }
  asm volatile("s_waitcnt vmcnt(16)" : "+v"(a4_0), "+v"(a4_1) :: "memory"); SB();
  acc[0][0] = MF16(w4_0, a4_0, acc[0][0]);
  acc[1][0] = MF16(w4_0, a4_1, acc[1][0]);
  acc[0][1] = MF16(w4_1, a4_0, acc[0][1]);
  acc[1][1] = MF16(w4_1, a4_1, acc[1][1]);
  SB();
  OPAQUE(wa3); const unsigned wb5 = wa3 + 81920u;
  bf16x8 w5_0 = ldtr((wb5 ^ 0u), (wb5 ^ 0u) + 512);
  bf16x8 w5_1 = ldtr((wb5 ^ 32u), (wb5 ^ 32u) + 512);
  bf16x8 w5_2 = ldtr((wb5 ^ 64u), (wb5 ^ 64u) + 512);
  bf16x8 w5_3 = ldtr((wb5 ^ 96u), (wb5 ^ 96u) + 512);
  SB();
  acc[0][2] = MF16(w4_2, a4_0, acc[0][2]);
  acc[1][2] = MF16(w4_2, a4_1, acc[1][2]);
  acc[0][3] = MF16(w4_3, a4_0, acc[0][3]);
  acc[1][3] = MF16(w4_3, a4_1, acc[1][3]);
  SB();
  bf16x8 a9_0, a9_1; { const unsigned so_ = soh + 2304;
    asm volatile("buffer_load_dwordx4 %0, %2, %4, %5 offen sc1\n\tbuffer_load_dwordx4 %1, %3, %4, %5 offen sc1" : "=&v"(a9_0), "=&v"(a9_1) : "v"(av[0]), "v"(av[1]), "s"(ars), "s"(so_) : "memory"); }
  asm volatile("s_waitcnt vmcnt(16)" : "+v"(a5_0), "+v"(a5_1) :: "memory"); SB();
  acc[0][0] = MF16(w5_0, a5_0, acc[0][0]);
  acc[1][0] = MF16(w5_0, a5_1, acc[1][0]);
  acc[0][1] = MF16(w5_1, a5_0, acc[0][1]);
  acc[1][1] = MF16(w5_1, a5_1, acc[1][1]);
  SB();
  OPAQUE(wa3); const unsigned wb6 = wa3 + 98304u;
  bf16x8 w6_0 = ldtr((wb6 ^ 0u), (wb6 ^ 0u) + 512);
  bf16x8 w6_1 = ldtr((wb6 ^ 32u), (wb6 ^ 32u) + 512);
  bf16x8 w6_2 = ldtr((wb6 ^ 64u), (wb6 ^ 64u) + 512);
  bf16x8 w6_3 = ldtr((wb6 ^ 96u), (wb6 ^ 96u) + 512);
  SB();
  acc[0][2] = MF16(w5_2, a5_0, acc[0][2]);
  acc[1][2] = MF16(w5_2, a5_1, acc[1][2]);
  acc[0][3] = MF16(w5_3, a5_0, acc[0][3]);
  acc[1][3] = MF16(w5_3, a5_1, acc[1][3]);
  SB();
  bf16x8 a10_0, a10_1; { const unsigned so_ = soh + 2560;
    asm volatile("buffer_load_dwordx4 %0, %2, %4, %5 offen sc1\n\tbuffer_load_dwordx4 %1, %3, %4, %5 offen sc1" : "=&v"(a10_0), "=&v"(a10_1) : "v"(av[0]), "v"(av[1]), "s"(ars), "s"(so_) : "memory"); }
  asm volatile("s_waitcnt vmcnt(16)" : "+v"(a6_0), "+v"(a6_1) :: "memory"); SB();
  acc[0][0] = MF16(w6_0, a6_0, acc[0][0]);
  acc[1][0] = MF16(w6_0, a6_1, acc[1][0]);
  acc[0][1] = MF16(w6_1, a6_0, acc[0][1]);
  acc[1][1] = MF16(w6_1, a6_1, acc[1][1]);
  SB();
  OPAQUE(wa3); const unsigned wb7 = wa3 + 114688u;
  bf16x8 w7_0 = ldtr((wb7 ^ 0u), (wb7 ^ 0u) + 512);
  bf16x8 w7_1 = ldtr((wb7 ^ 32u), (wb7 ^ 32u) + 512);
  bf16x8 w7_2 = ldtr((wb7 ^ 64u), (wb7 ^ 64u) + 512);
  bf16x8 w7_3 = ldtr((wb7 ^ 96u), (wb7 ^ 96u) + 512);
  SB();
  acc[0][2] = MF16(w6_2, a6_0, acc[0][2]);
  acc[1][2] = MF16(w6_2, a6_1, acc[1][2]);
  acc[0][3] = MF16(w6_3, a6_0, acc[0][3]);
  acc[1][3] = MF16(w6_3, a6_1, acc[1][3]);
  SB();
  bf16x8 a11_0, a11_1; { const unsigned so_ = soh + 2816;
    asm volatile("buffer_load_dwordx4 %0, %2, %4, %5 offen sc1\n\tbuffer_load_dwordx4 %1, %3, %4, %5 offen sc1" : "=&v"(a11_0), "=&v"(a11_1) : "v"(av[0]), "v"(av[1]), "s"(ars), "s"(so_) : "memory"); }
  asm volatile("s_waitcnt vmcnt(16)" : "+v"(a7_0), "+v"(a7_1) :: "memory"); SB();
  acc[0][0] = MF16(w7_0, a7_0, acc[0][0]);
  acc[1][0] = MF16(w7_0, a7_1, acc[1][0]);
  acc[0][1] = MF16(w7_1, a7_0, acc[0][1]);
  acc[1][1] = MF16(w7_1, a7_1, acc[1][1]);
  SB();
  { const int l_ = lane & 15; const int pn_ = (l_ & 8) ? (r0 + nr3 - 1) >> 7 : r0 >> 7; GAS u64* fp = flagO + pn_ * 8 + (l_ & 7); POLL(fp, w); }
  TS(12);
  asm volatile("s_waitcnt vmcnt(8) lgkmcnt(0)\n\ts_barrier" ::: "memory"); SB();
  W3_LOADH(13, 49152)
  W3_LOADH(14, 81920)
  W3_LOADH(15, 98304)
  W3_LOADH(16, 114688)
  OPAQUE(wa3); const unsigned wb8 = wa3 + 131072u;
  bf16x8 w8_0 = ldtr((wb8 ^ 0u), (wb8 ^ 0u) + 512);
  bf16x8 w8_1 = ldtr((wb8 ^ 32u), (wb8 ^ 32u) + 512);
  bf16x8 w8_2 = ldtr((wb8 ^ 64u), (wb8 ^ 64u) + 512);
  bf16x8 w8_3 = ldtr((wb8 ^ 96u), (wb8 ^ 96u) + 512);
  SB();
  acc[0][2] = MF16(w7_2, a7_0, acc[0][2]);
  acc[1][2] = MF16(w7_2, a7_1, acc[1][2]);
  acc[0][3] = MF16(w7_3, a7_0, acc[0][3]);
  acc[1][3] = MF16(w7_3, a7_1, acc[1][3]);
  SB();
  bf16x8 a12_0, a12_1; { const unsigned so_ = soo + 0;
    asm volatile("buffer_load_dwordx4 %0, %2, %4, %5 offen sc1\n\tbuffer_load_dwordx4 %1, %3, %4, %5 offen sc1" : "=&v"(a12_0), "=&v"(a12_1) : "v"(av[0]), "v"(av[1]), "s"(ars), "s"(so_) : "memory"); }
  asm volatile("s_waitcnt vmcnt(16)" : "+v"(a8_0), "+v"(a8_1) :: "memory"); SB();
  acc[0][0] = MF16(w8_0, a8_0, acc[0][0]);
  acc[1][0] = MF16(w8_0, a8_1, acc[1][0]);
  acc[0][1] = MF16(w8_1, a8_0, acc[0][1]);
  acc[1][1] = MF16(w8_1, a8_1, acc[1][1]);
  SB();
  OPAQUE(wa3); const unsigned wb9 = wa3 + 65536u;
  bf16x8 w9_0 = ldtr((wb9 ^ 0u), (wb9 ^ 0u) + 512);
  bf16x8 w9_1 = ldtr((wb9 ^ 32u), (wb9 ^ 32u) + 512);
  bf16x8 w9_2 = ldtr((wb9 ^ 64u), (wb9 ^ 64u) + 512);
  bf16x8 w9_3 = ldtr((wb9 ^ 96u), (wb9 ^ 96u) + 512);
  SB();
  acc[0][2] = MF16(w8_2, a8_0, acc[0][2]);
  acc[1][2] = MF16(w8_2, a8_1, acc[1][2]);
  acc[0][3] = MF16(w8_3, a8_0, acc[0][3]);
  acc[1][3] = MF16(w8_3, a8_1, acc[1][3]);
  SB();
  bf16x8 a13_0, a13_1; { const unsigned so_ = soo + 256;
    asm volatile("buffer_load_dwordx4 %0, %2, %4, %5 offen sc1\n\tbuffer_load_dwordx4 %1, %3, %4, %5 offen sc1" : "=&v"(a13_0), "=&v"(a13_1) : "v"(av[0]), "v"(av[1]), "s"(ars), "s"(so_) : "memory"); }
  asm volatile("s_waitcnt vmcnt(16)" : "+v"(a9_0), "+v"(a9_1) :: "memory"); SB();
  acc[0][0] = MF16(w9_0, a9_0, acc[0][0]);
  acc[1][0] = MF16(w9_0, a9_1, acc[1][0]);
  acc[0][1] = MF16(w9_1, a9_0, acc[0][1]);
  acc[1][1] = MF16(w9_1, a9_1, acc[1][1]);
  SB();
  OPAQUE(wa3); const unsigned wb10 = wa3 + 0u;
  bf16x8 w10_0 = ldtr((wb10 ^ 0u), (wb10 ^ 0u) + 512);
  bf16x8 w10_1 = ldtr((wb10 ^ 32u), (wb10 ^ 32u) + 512);
  bf16x8 w10_2 = ldtr((wb10 ^ 64u), (wb10 ^ 64u) + 512);
  bf16x8 w10_3 = ldtr((wb10 ^ 96u), (wb10 ^ 96u) + 512);
  SB();
  acc[0][2] = MF16(w9_2, a9_0, acc[0][2]);
  acc[1][2] = MF16(w9_2, a9_1, acc[1][2]);
  acc[0][3] = MF16(w9_3, a9_0, acc[0][3]);
  acc[1][3] = MF16(w9_3, a9_1, acc[1][3]);
  SB();
  bf16x8 a14_0, a14_1; { const unsigned so_ = soo + 512;
    asm volatile("buffer_load_dwordx4 %0, %2, %4, %5 offen sc1\n\tbuffer_load_dwordx4 %1, %3, %4, %5 offen sc1" : "=&v"(a14_0), "=&v"(a14_1) : "v"(av[0]), "v"(av[1]), "s"(ars), "s"(so_) : "memory"); }
  asm volatile("s_waitcnt vmcnt(16)" : "+v"(a10_0), "+v"(a10_1) :: "memory"); SB();
  acc[0][0] = MF16(w10_0, a10_0, acc[0][0]);
  acc[1][0] = MF16(w10_0, a10_1, acc[1][0]);
  acc[0][1] = MF16(w10_1, a10_0, acc[0][1]);
  acc[1][1] = MF16(w10_1, a10_1, acc[1][1]);
  SB();
  OPAQUE(wa3); const unsigned wb11 = wa3 + 16384u;
  bf16x8 w11_0 = ldtr((wb11 ^ 0u), (wb11 ^ 0u) + 512);
  bf16x8 w11_1 = ldtr((wb11 ^ 32u), (wb11 ^ 32u) + 512);
  bf16x8 w11_2 = ldtr((wb11 ^ 64u), (wb11 ^ 64u) + 512);
  bf16x8 w11_3 = ldtr((wb11 ^ 96u), (wb11 ^ 96u) + 512);
  SB();
  acc[0][2] = MF16(w10_2, a10_0, acc[0][2]);
  acc[1][2] = MF16(w10_2, a10_1, acc[1][2]);
  acc[0][3] = MF16(w10_3, a10_0, acc[0][3]);
  acc[1][3] = MF16(w10_3, a10_1, acc[1][3]);
  SB();
  bf16x8 a15_0, a15_1; { const unsigned so_ = soo + 768;
    asm volatile("buffer_load_dwordx4 %0, %2, %4, %5 offen sc1\n\tbuffer_load_dwordx4 %1, %3, %4, %5 offen sc1" : "=&v"(a15_0), "=&v"(a15_1) : "v"(av[0]), "v"(av[1]), "s"(ars), "s"(so_) : "memory"); }
  asm volatile("s_waitcnt vmcnt(16)" : "+v"(a11_0), "+v"(a11_1) :: "memory"); SB();
  acc[0][0] = MF16(w11_0, a11_0, acc[0][0]);
  acc[1][0] = MF16(w11_0, a11_1, acc[1][0]);
  acc[0][1] = MF16(w11_1, a11_0, acc[0][1]);
  acc[1][1] = MF16(w11_1, a11_1, acc[1][1]);
  SB();
  OPAQUE(wa3); const unsigned wb12 = wa3 + 32768u;
  bf16x8 w12_0 = ldtr((wb12 ^ 0u), (wb12 ^ 0u) + 512);
  bf16x8 w12_1 = ldtr((wb12 ^ 32u), (wb12 ^ 32u) + 512);
  bf16x8 w12_2 = ldtr((wb12 ^ 64u), (wb12 ^ 64u) + 512);
  bf16x8 w12_3 = ldtr((wb12 ^ 96u), (wb12 ^ 96u) + 512);
  SB();
  acc[0][2] = MF16(w11_2, a11_0, acc[0][2]);
  acc[1][2] = MF16(w11_2, a11_1, acc[1][2]);
  acc[0][3] = MF16(w11_3, a11_0, acc[0][3]);
  acc[1][3] = MF16(w11_3, a11_1, acc[1][3]);
  SB();
  bf16x8 a16_0, a16_1; { const unsigned so_ = soo + 1024;
    asm volatile("buffer_load_dwordx4 %0, %2, %4, %5 offen sc1\n\tbuffer_load_dwordx4 %1, %3, %4, %5 offen sc1" : "=&v"(a16_0), "=&v"(a16_1) : "v"(av[0]), "v"(av[1]), "s"(ars), "s"(so_) : "memory"); }
  asm volatile("s_waitcnt vmcnt(8)" : "+v"(a12_0), "+v"(a12_1) :: "memory"); SB();
  acc[0][0] = MF16(w12_0, a12_0, acc[0][0]);
  acc[1][0] = MF16(w12_0, a12_1, acc[1][0]);
  acc[0][1] = MF16(w12_1, a12_0, acc[0][1]);
  acc[1][1] = MF16(w12_1, a12_1, acc[1][1]);
  SB();
  asm volatile("s_waitcnt vmcnt(10) lgkmcnt(0)\n\ts_barrier" ::: "memory"); SB();
  OPAQUE(wa3); const unsigned wb13 = wa3 + 49152u;
  bf16x8 w13_0 = ldtr((wb13 ^ 0u), (wb13 ^ 0u) + 512);
  bf16x8 w13_1 = ldtr((wb13 ^ 32u), (wb13 ^ 32u) + 512);
  bf16x8 w13_2 = ldtr((wb13 ^ 64u), (wb13 ^ 64u) + 512);
  bf16x8 w13_3 = ldtr((wb13 ^ 96u), (wb13 ^ 96u) + 512);
  SB();
  acc[0][2] = MF16(w12_2, a12_0, acc[0][2]);
  acc[1][2] = MF16(w12_2, a12_1, acc[1][2]);
  acc[0][3] = MF16(w12_3, a12_0, acc[0][3]);
  acc[1][3] = MF16(w12_3, a12_1, acc[1][3]);
  SB();
  FINAL_PREFETCH
  asm volatile("s_waitcnt vmcnt(9)" : "+v"(a13_0), "+v"(a13_1) :: "memory"); SB();
  acc[0][0] = MF16(w13_0, a13_0, acc[0][0]);
  acc[1][0] = MF16(w13_0, a13_1, acc[1][0]);
  acc[0][1] = MF16(w13_1, a13_0, acc[0][1]);
  acc[1][1] = MF16(w13_1, a13_1, acc[1][1]);
  SB();
  OPAQUE(wa3); const unsigned wb14 = wa3 + 81920u;
  bf16x8 w14_0 = ldtr((wb14 ^ 0u), (wb14 ^ 0u) + 512);
  bf16x8 w14_1 = ldtr((wb14 ^ 32u), (wb14 ^ 32u) + 512);
  bf16x8 w14_2 = ldtr((wb14 ^ 64u), (wb14 ^ 64u) + 512);
  bf16x8 w14_3 = ldtr((wb14 ^ 96u), (wb14 ^ 96u) + 512);
  SB();
  acc[0][2] = MF16(w13_2, a13_0, acc[0][2]);
  acc[1][2] = MF16(w13_2, a13_1, acc[1][2]);
  acc[0][3] = MF16(w13_3, a13_0, acc[0][3]);
  acc[1][3] = MF16(w13_3, a13_1, acc[1][3]);
  SB();
  asm volatile("s_waitcnt vmcnt(7)" : "+v"(a14_0), "+v"(a14_1) :: "memory"); SB();
  acc[0][0] = MF16(w14_0, a14_0, acc[0][0]);
  acc[1][0] = MF16(w14_0, a14_1, acc[1][0]);
  acc[0][1] = MF16(w14_1, a14_0, acc[0][1]);
  acc[1][1] = MF16(w14_1, a14_1, acc[1][1]);
  SB();
  OPAQUE(wa3); const unsigned wb15 = wa3 + 98304u;
  bf16x8 w15_0 = ldtr((wb15 ^ 0u), (wb15 ^ 0u) + 512);
  bf16x8 w15_1 = ldtr((wb15 ^ 32u), (wb15 ^ 32u) + 512);
  bf16x8 w15_2 = ldtr((wb15 ^ 64u), (wb15 ^ 64u) + 512);
  bf16x8 w15_3 = ldtr((wb15 ^ 96u), (wb15 ^ 96u) + 512);
  SB();
  acc[0][2] = MF16(w14_2, a14_0, acc[0][2]);
  acc[1][2] = MF16(w14_2, a14_1, acc[1][2]);
  acc[0][3] = MF16(w14_3, a14_0, acc[0][3]);
  acc[1][3] = MF16(w14_3, a14_1, acc[1][3]);
  SB();
  asm volatile("s_waitcnt vmcnt(5)" : "+v"(a15_0), "+v"(a15_1) :: "memory"); SB();
  acc[0][0] = MF16(w15_0, a15_0, acc[0][0]);
  acc[1][0] = MF16(w15_0, a15_1, acc[1][0]);
  acc[0][1] = MF16(w15_1, a15_0, acc[0][1]);
  acc[1][1] = MF16(w15_1, a15_1, acc[1][1]);
  SB();
  OPAQUE(wa3); const unsigned wb16 = wa3 + 114688u;
  bf16x8 w16_0 = ldtr((wb16 ^ 0u), (wb16 ^ 0u) + 512);
  bf16x8 w16_1 = ldtr((wb16 ^ 32u), (wb16 ^ 32u) + 512);
  bf16x8 w16_2 = ldtr((wb16 ^ 64u), (wb16 ^ 64u) + 512);
  bf16x8 w16_3 = ldtr((wb16 ^ 96u), (wb16 ^ 96u) + 512);
  SB();
  acc[0][2] = MF16(w15_2, a15_0, acc[0][2]);
  acc[1][2] = MF16(w15_2, a15_1, acc[1][2]);
  acc[0][3] = MF16(w15_3, a15_0, acc[0][3]);
  acc[1][3] = MF16(w15_3, a15_1, acc[1][3]);
  SB();
  asm volatile("s_waitcnt vmcnt(3)" : "+v"(a16_0), "+v"(a16_1) :: "memory"); SB();
  acc[0][0] = MF16(w16_0, a16_0, acc[0][0]);
  acc[1][0] = MF16(w16_0, a16_1, acc[1][0]);
  acc[0][1] = MF16(w16_1, a16_0, acc[0][1]);
  acc[1][1] = MF16(w16_1, a16_1, acc[1][1]);
  SB();
  SB();
  acc[0][2] = MF16(w16_2, a16_0, acc[0][2]);
  acc[1][2] = MF16(w16_2, a16_1, acc[1][2]);
  acc[0][3] = MF16(w16_3, a16_0, acc[0][3]);
  acc[1][3] = MF16(w16_3, a16_1, acc[1][3]);
  SB();
  TS(13);
  asm volatile("s_waitcnt vmcnt(0)\n\ts_barrier" : "+v"(xv0_), "+v"(xv1_), "+v"(bv_) :: "memory"); xv0 = xv0_; xv1 = xv1_; bv = bv_;
  *(LAS f32x4*)(pa + 0) = acc[0][0];
  *(LAS f32x4*)(pa + 64) = acc[0][1];
  *(LAS f32x4*)(pa + 128) = acc[0][2];
  *(LAS f32x4*)(pa + 192) = acc[0][3];
  *(LAS f32x4*)(pa + 4352) = acc[1][0];
  *(LAS f32x4*)(pa + 4416) = acc[1][1];
  *(LAS f32x4*)(pa + 4480) = acc[1][2];
  *(LAS f32x4*)(pa + 4544) = acc[1][3];
  }

  asm volatile("s_waitcnt lgkmcnt(0)\n\ts_barrier" ::: "memory");
#pragma unroll
  for (int i = 0; i < 2; ++i) {
    const int c = i * 512 + (int)threadIdx.x; const int tl = c >> 3, c8 = c & 7;
    if (tl < nr3 && p3valid) {
      const v4u xv = i ? xv1 : xv0;
      f32x4 s0 = {0.f, 0.f, 0.f, 0.f}, s1 = {0.f, 0.f, 0.f, 0.f};
#pragma unroll
      for (int k = 0; k < 4; ++k) { const unsigned a_ = lb + (k * 96 + tl) * 272 + c8 * 32; s0 += ldf4(a_); s1 += ldf4(a_ + 16); }
      v4u ov;
#pragma unroll
      for (int k = 0; k < 4; ++k) { const unsigned xu = xv[k], bu = bv[k];
        const float lo = (k < 2 ? s0[2 * k] : s1[2 * k - 4]) + __uint_as_float(xu << 16) + __uint_as_float(bu << 16);
        const float hi = (k < 2 ? s0[2 * k + 1] : s1[2 * k - 3]) + __uint_as_float(xu & 0xffff0000u) + __uint_as_float(bu & 0xffff0000u);
        f32x2 t_ = {lo, hi}; ov[k] = pk2(t_); }
      __builtin_nontemporal_store(ov, (v4u*)(out + (size_t)(r0 + tl) * 640 + ct3 * 64 + c8 * 8));
    }
  }
  if (blockIdx.x == 0) {
    { GAS u64* fp = flagO + (threadIdx.x & 255); POLL(fp, rp); }
    asm volatile("s_barrier" ::: "memory");
    if (threadIdx.x == 0) __hip_atomic_store((GAS u64*)(ws + WS_SYNC_OFF), tag, __ATOMIC_RELAXED, __HIP_MEMORY_SCOPE_AGENT);
  }

  TS(6);
}
extern "C" void kernel_launch(void* const* d_in, const int* in_sizes, int n_in, void* d_out, int out_size, void* d_ws, size_t ws_size, hipStream_t stream) {
  static const Tab tab = {{270009345u, 270019593u, 274203665u, 274213913u, 278398465u, 278408713u, 282592785u, 282603033u, 272106501u, 272116749u, 276300821u, 276311069u, 280495621u, 280505869u, 284689941u, 284700189u, 402654211u, 402664459u, 406848531u, 406858779u, 411043331u, 411053579u, 415237651u, 415247899u, 403440647u, 403450895u, 407634967u, 407645215u, 411829767u, 411840015u, 416024087u, 416034335u, 270011457u, 270021705u, 274205777u, 274216025u, 278400577u, 278410825u, 282594897u, 282605145u, 272108613u, 272118861u, 276302933u, 276313181u, 280497733u, 280507981u, 284692053u, 284702301u, 404751427u, 404761675u, 408945747u, 408955995u, 413140547u, 413150795u, 417334867u, 417345115u, 405537863u, 405548111u, 409732183u, 409742431u, 413926983u, 413937231u, 418121303u, 418131551u, 270013569u, 270023817u, 274207889u, 274218137u, 278402689u, 278412937u, 282597009u, 282607257u, 272110725u, 272120973u, 276305045u, 276315293u, 280499845u, 280510093u, 284694165u, 284704413u, 402656387u, 402666635u, 406850707u, 406860955u, 411045507u, 411055755u, 415239827u, 415250075u, 403442823u, 403453071u, 407637143u, 407647391u, 411831943u, 411842191u, 416026263u, 416036511u, 268442817u, 268453065u, 272637137u, 272647385u, 276831937u, 276842185u, 281026257u, 281036505u, 269753541u, 269763789u, 273947861u, 273958109u, 278142661u, 278152909u, 282336981u, 282347229u, 404753603u, 404763851u, 408947923u, 408958171u, 413142723u, 413152971u, 417337043u, 417347291u, 405540039u, 405550287u, 409734359u, 409744607u, 413929159u, 413939407u, 418123479u, 418133727u, 270277889u, 270288137u, 274472209u, 274482457u, 278667009u, 278677257u, 282861329u, 282871577u, 271588613u, 271598861u, 275782933u, 275793181u, 279977733u, 279987981u, 284172053u, 284182301u, 402658563u, 402668811u, 406852883u, 406863131u, 411047683u, 411057931u, 415242003u, 415252251u, 403444999u, 403455247u, 407639319u, 407649567u, 411834119u, 411844367u, 416028439u, 416038687u, 272112961u, 272123209u, 276307281u, 276317529u, 280502081u, 280512329u, 284696401u, 284706649u, 268444997u, 268455245u, 272639317u, 272649565u, 276834117u, 276844365u, 281028437u, 281038685u, 404755779u, 404766027u, 408950099u, 408960347u, 413144899u, 413155147u, 417339219u, 417349467u, 405542215u, 405552463u, 409736535u, 409746783u, 413931335u, 413941583u, 418125655u, 418135903u, 269755777u, 269766025u, 273950097u, 273960345u, 278144897u, 278155145u, 282339217u, 282349465u, 270280069u, 270290317u, 274474389u, 274484637u, 278669189u, 278679437u, 282863509u, 282873757u, 403185027u, 403195275u, 407379347u, 407389595u, 411574147u, 411584395u, 415768467u, 415778715u, 405020039u, 405030287u, 409214359u, 409224607u, 413409159u, 413419407u, 417603479u, 417613727u, 271590849u, 271601097u, 275785169u, 275795417u, 279979969u, 279990217u, 284174289u, 284184537u, 272115141u, 272125389u, 276309461u, 276319709u, 280504261u, 280514509u, 284698581u, 284708829u, 403187139u, 403197387u, 407381459u, 407391707u, 411576259u, 411586507u, 415770579u, 415780827u, 405022151u, 405032399u, 409216471u, 409226719u, 413411271u, 413421519u, 417605591u, 417615839u}};
  fused_kernel<<<256, 512, 0, stream>>>((const u16*)d_in[0], (const u16*)d_in[1], (const u16*)d_in[2], (const u16*)d_in[4], (const u16*)d_in[5], (const u16*)d_in[6], (const u16*)d_in[7], (u16*)d_out, (char*)d_ws, tab);
}
```
